```python
import torch

def _softmax(x, axis=-1):
    return torch.softmax(x, dim=axis)
def _gelu(x):
    return torch.nn.functional.gelu(x, approximate="tanh")
def _silu(x):
    return torch.nn.functional.silu(x)
def _sigmoid(x):
    return torch.sigmoid(x)
def _layernorm(x, axis=-1, eps=1e-5):
    m = x.mean(dim=axis, keepdim=True)
    v = x.var(dim=axis, keepdim=True, unbiased=False)
    return (x - m) / torch.sqrt(v + eps)
def _scatter_add(tgt, idx, src, axis=0):
    return tgt.clone().index_add_(axis, idx, src)
def _segment_sum(src, seg, n):
    out = torch.zeros((n,) + src.shape[1:], dtype=src.dtype)
    return out.index_add_(0, seg, src)
def _pad(x, pw):
    flat = tuple(v for pair in reversed(pw) for v in pair)
    return torch.nn.functional.pad(x, flat)

kernel_name = 'sequential_preLN__mha_block_cached__swiglu_ffn__decode'

def setup_inputs():

    _w = (lambda a: (lambda t: t.to(torch.bfloat16) if t.is_floating_point() else t)(torch.as_tensor(a)))
    def _A_setup():
        B, Sq, D = 8, 1, 640
        H, Sk, Dh = 32, 4096, 128
        return {
            "x": _w((lambda *s: torch.randn(*s, dtype=torch.bfloat16) * 0.05)(B, Sq, D)),
            "k_cache": _w((lambda *s: torch.randn(*s, dtype=torch.bfloat16) * 3.2)(B, H, Sk, Dh)),
            "v_cache": _w((lambda *s: torch.randn(*s, dtype=torch.bfloat16) * 0.4)(B, H, Sk, Dh)),
            "wq": _w((lambda *s: torch.randn(*s, dtype=torch.bfloat16) * 3.2)(D, H * Dh) * (D ** -0.5)),
            "wo": _w((lambda *s: torch.randn(*s, dtype=torch.bfloat16) * 0.4)(H * Dh, D) * ((H * Dh) ** -0.5)),
        }
    _A_in = _A_setup()

    def _B_setup():
        B, S, D, F = 8, 1, 640, 1728
        return {
            "x": _w((lambda *s: torch.randn(*s, dtype=torch.bfloat16) * 0.05)(B, S, D)),
            "w_gate": _w((lambda *s: torch.randn(*s, dtype=torch.bfloat16) * 0.05)(D, F)),
            "w_up": _w((lambda *s: torch.randn(*s, dtype=torch.bfloat16) * 0.05)(D, F)),
            "w_down": _w((lambda *s: torch.randn(*s, dtype=torch.bfloat16) * 0.05)(F, D)),
        }
    _B_in = _B_setup()

    return {
        "A_x": _A_in["x"],
        "A_k_cache": _A_in["k_cache"],
        "A_v_cache": _A_in["v_cache"],
        "A_wq": _A_in["wq"],
        "A_wo": _A_in["wo"],
        "B_x": _B_in["x"],
        "B_w_gate": _B_in["w_gate"],
        "B_w_up": _B_in["w_up"],
        "B_w_down": _B_in["w_down"],
    }

def reference(A_x, A_k_cache, A_v_cache, A_wq, A_wo, B_x, B_w_gate, B_w_up, B_w_down):
    def _A_ref(x, k_cache, v_cache, wq, wo):
        B, Sq, D = x.shape
        H, Dh = 32, 128
        q = (lambda x, *d: x.permute(*d))((x @ wq).reshape(B, Sq, H, Dh), 0, 2, 1, 3)
        scores = torch.einsum("bhqd,bhkd->bhqk", q, k_cache) * (Dh ** -0.5)
        p = _softmax(scores, axis=-1)
        o = torch.einsum("bhqk,bhkd->bhqd", p, v_cache)
        return (lambda x, *d: x.permute(*d))(o, 0, 2, 1, 3).reshape(B, Sq, H * Dh) @ wo

    def _B_ref(x, w_gate, w_up, w_down):
        g = _silu(x @ w_gate)
        u = x @ w_up
        return (g * u) @ w_down

    x = A_x
    h = x + _A_ref(_layernorm(x, axis=-1), A_k_cache, A_v_cache, A_wq, A_wo)
    h2 = h + _B_ref(_layernorm(h, axis=-1), B_w_gate, B_w_up, B_w_down)
    return h2
```

```cpp
#include <hip/hip_runtime.h>
#include <hip/hip_bf16.h>
#include <cstdint>

constexpr int B = 8, D = 640, H = 32, SK = 4096, DH = 128, F = 1728;
using u32x4 = __attribute__((ext_vector_type(4))) unsigned;
using bf16x2 = __attribute__((ext_vector_type(2))) __bf16;

__device__ __forceinline__ float dot2(unsigned a, unsigned b, float acc) {
  return __builtin_amdgcn_fdot2_f32_bf16(__builtin_bit_cast(bf16x2, a), __builtin_bit_cast(bf16x2, b), acc, false);
}
__device__ __forceinline__ float bf2f(unsigned short u) { return __builtin_bit_cast(float, ((unsigned)u) << 16); }
__device__ __forceinline__ unsigned short f2bf(float f) { return __builtin_bit_cast(unsigned short, (__bf16)f); }
__device__ __forceinline__ float rbf(float f) { return bf2f(f2bf(f)); }
template <int CTRL> __device__ __forceinline__ float dpp(float x) {
  return __builtin_bit_cast(float, __builtin_amdgcn_mov_dpp(__builtin_bit_cast(int, x), CTRL, 0xf, 0xf, true));
}
template <int CTRL> __device__ __forceinline__ unsigned dppu(unsigned x) {
  return (unsigned)__builtin_amdgcn_mov_dpp((int)x, CTRL, 0xf, 0xf, true);
}
constexpr int XOR1 = 0xB1, XOR2 = 0x4E, XOR7 = 0x141, XOR8 = 0x128, ROR4 = 0x124;
__device__ __forceinline__ float xrow_max(float x) {
  auto s = __builtin_amdgcn_permlane16_swap(__float_as_uint(x), __float_as_uint(x), false, false);
  x = fmaxf(__uint_as_float(s[0]), __uint_as_float(s[1]));
  auto t = __builtin_amdgcn_permlane32_swap(__float_as_uint(x), __float_as_uint(x), false, false);
  return fmaxf(__uint_as_float(t[0]), __uint_as_float(t[1]));
}
__device__ __forceinline__ float xrow_sum(float x) {
  auto s = __builtin_amdgcn_permlane16_swap(__float_as_uint(x), __float_as_uint(x), false, false);
  x = __uint_as_float(s[0]) + __uint_as_float(s[1]);
  auto t = __builtin_amdgcn_permlane32_swap(__float_as_uint(x), __float_as_uint(x), false, false);
  return __uint_as_float(t[0]) + __uint_as_float(t[1]);
}
__device__ __forceinline__ float wave_sum(float x) {
  x += dpp<XOR1>(x); x += dpp<XOR2>(x); x += dpp<XOR7>(x); x += dpp<XOR8>(x);
  return xrow_sum(x);
}
__device__ __forceinline__ float wave_max(float x) {
  x = fmaxf(x, dpp<XOR1>(x)); x = fmaxf(x, dpp<XOR2>(x)); x = fmaxf(x, dpp<XOR7>(x)); x = fmaxf(x, dpp<XOR8>(x));
  return xrow_max(x);
}

__device__ __forceinline__ void ln_row(float (&v)[10]) {
  float s = 0.f;
#pragma unroll
  for (int i = 0; i < 10; ++i) s += v[i];
  s = wave_sum(s);
  const float mean = s * (1.f / D);
  float ss = 0.f;
#pragma unroll
  for (int i = 0; i < 10; ++i) { float d = v[i] - mean; ss += d * d; }
  ss = wave_sum(ss);
  const float mb = rbf(mean), vb = rbf(ss * (1.f / D));
  const float sd = rbf(sqrtf(rbf(vb + 1e-5f)));
#pragma unroll
  for (int i = 0; i < 10; ++i) v[i] = rbf(rbf(v[i] - mb) / sd);
}

constexpr int NW = 8, WIN = 16, NBT = SK / (4 * NW * 16), NWQ = D / (4 * NW);
__global__ void __launch_bounds__(NW * 64, NW / 4) attn_kernel(const unsigned short* __restrict__ x, const unsigned short* __restrict__ kc,
                                                     const unsigned short* __restrict__ vc, const unsigned short* __restrict__ wq,
                                                     unsigned short* __restrict__ obuf) {
  __shared__ __attribute__((aligned(16))) float lds[NW * 128 + 640 + 128 + 64];
  float* red = lds;
  unsigned* xn = (unsigned*)(lds + NW * 128);
  unsigned short* qb = (unsigned short*)(lds + NW * 128 + 640);
  float* ml = lds + NW * 128 + 640 + 128;
  const int bh = blockIdx.x, b = bh >> 5, h = bh & 31;
  const int tid = threadIdx.x, lane = tid & 63, wave = __builtin_amdgcn_readfirstlane(tid >> 6), r = lane >> 4, c = lane & 15;

  u32x4 w[NWQ];
  const __amdgpu_buffer_rsrc_t wrs = __builtin_amdgcn_make_buffer_rsrc((void*)(wq + h * DH), 0, D * H * DH * 2 - h * DH * 2, 0x00020000);
#pragma unroll
  for (int i = 0; i < NWQ; ++i) w[i] = __builtin_amdgcn_raw_buffer_load_b128(wrs, (wave * 4 + r) * (H * DH * 2) + 16 * c, i * 4 * NW * (H * DH * 2), 0);
  float xv[10];
  if (wave == 0) {
#pragma unroll
    for (int i = 0; i < 10; ++i) xv[i] = bf2f(x[b * D + lane + 64 * i]);
  }
  const __amdgpu_buffer_rsrc_t krs = __builtin_amdgcn_make_buffer_rsrc((void*)(kc + (size_t)bh * SK * DH), 0, SK * DH * 2, 0x00020000);
  const __amdgpu_buffer_rsrc_t vrs = __builtin_amdgcn_make_buffer_rsrc((void*)(vc + (size_t)bh * SK * DH), 0, SK * DH * 2, 0x00020000);
  const int voff = tid * 16;
#define LDK(i) __builtin_amdgcn_raw_buffer_load_b128(krs, voff, (i) * (NW * 1024), 2)
#define LDV(i) __builtin_amdgcn_raw_buffer_load_b128(vrs, voff, (i) * (NW * 1024), 2)
  u32x4 buf[WIN];
#pragma unroll
  for (int i = 0; i < WIN; ++i) buf[i] = LDK(i);
  __builtin_amdgcn_sched_barrier(0);
  if (wave == 0) {
    ln_row(xv);
#pragma unroll
    for (int i = 0; i < 10; ++i) xn[lane + 64 * i] = f2bf(xv[i]);
  }
  __syncthreads();
  {
    float acc[8];
#pragma unroll
    for (int j = 0; j < 8; ++j) acc[j] = 0.f;
#pragma unroll
    for (int i = 0; i < NWQ; ++i) {
      const unsigned lo = xn[i * 4 * NW + wave * 4 + r], hi = lo << 16;
#pragma unroll
      for (int j = 0; j < 4; ++j) { acc[2 * j] = dot2(w[i][j], lo, acc[2 * j]); acc[2 * j + 1] = dot2(w[i][j], hi, acc[2 * j + 1]); }
    }
#pragma unroll
    for (int j = 0; j < 8; ++j) acc[j] = xrow_sum(acc[j]);
    if (r == 0) {
#pragma unroll
      for (int j = 0; j < 8; ++j) red[wave * 128 + 8 * c + j] = acc[j];
    }
  }
  __syncthreads();
  if (tid < 128) {
    float s = 0.f;
#pragma unroll
    for (int i = 0; i < NW; ++i) s += red[i * 128 + tid];
    qb[tid] = f2bf(s);
  }
  __syncthreads();
  const u32x4 q = *reinterpret_cast<const u32x4*>(qb + 8 * c);
  float sc[NBT];
#pragma unroll
  for (int bt = 0; bt < NBT; ++bt) {
    float xx[16];
#pragma unroll
    for (int i = 0; i < 16; ++i) {
      const u32x4 k = buf[i];
      float d = dot2(k[0], q[0], 0.f); d = dot2(k[1], q[1], d); d = dot2(k[2], q[2], d); d = dot2(k[3], q[3], d);
      xx[i] = d;
      buf[i] = (bt < NBT - 1) ? LDK((bt + 1) * 16 + i) : LDV(i);
      __builtin_amdgcn_sched_barrier(0);
    }
#define TR_STEP(HALF, CTRL, BIT) _Pragma("unroll") for (int hh = 0; hh < (HALF); ++hh) { \
      const float lo_ = xx[hh], hi_ = xx[hh + (HALF)]; const float t1_ = lo_ + dpp<CTRL>(lo_), t2_ = hi_ + dpp<CTRL>(hi_); \
      xx[hh] = (c & (BIT)) ? t2_ : t1_; }
    TR_STEP(8, XOR8, 8) TR_STEP(4, XOR7, 4) TR_STEP(2, XOR2, 2) TR_STEP(1, XOR1, 1)
#undef TR_STEP
    sc[bt] = rbf(rbf(xx[0]) * 0.08838834764831845f);
  }
  float m = sc[0];
#pragma unroll
  for (int bt = 1; bt < NBT; ++bt) m = fmaxf(m, sc[bt]);
  m = wave_max(m);
  float l = 0.f;
#pragma unroll
  for (int bt = 0; bt < NBT; ++bt) l += __expf(sc[bt] - m);
  l = wave_sum(l);
  if (lane == 0) { ml[2 * wave] = m; ml[2 * wave + 1] = l; }
  asm volatile("s_waitcnt lgkmcnt(0)" ::: "memory");
  __builtin_amdgcn_s_barrier();
  float M = -INFINITY;
#pragma unroll
  for (int i = 0; i < NW; ++i) M = fmaxf(M, ml[2 * i]);
  float L = 0.f;
#pragma unroll
  for (int i = 0; i < NW; ++i) L += ml[2 * i + 1] * __expf(ml[2 * i] - M);
  const float inv = 1.f / L;
  unsigned pl[NBT];
#pragma unroll
  for (int bt = 0; bt < NBT; ++bt) pl[bt] = f2bf(__expf(sc[bt] - M) * inv);
  float acc[8];
#pragma unroll
  for (int j = 0; j < 8; ++j) acc[j] = 0.f;
#pragma unroll
  for (int bt = 0; bt < NBT; ++bt) {
#define VSTEP(i) { const u32x4 v = buf[i]; const unsigned lo = dppu<0x150 + i>(pl[bt]), hi = lo << 16; \
      _Pragma("unroll") for (int j = 0; j < 4; ++j) { acc[2 * j] = dot2(v[j], lo, acc[2 * j]); acc[2 * j + 1] = dot2(v[j], hi, acc[2 * j + 1]); } \
      if (bt < NBT - 1) buf[i] = LDV((bt + 1) * 16 + i); \
      __builtin_amdgcn_sched_barrier(0); }
    VSTEP(0) VSTEP(1) VSTEP(2) VSTEP(3) VSTEP(4) VSTEP(5) VSTEP(6) VSTEP(7)
    VSTEP(8) VSTEP(9) VSTEP(10) VSTEP(11) VSTEP(12) VSTEP(13) VSTEP(14) VSTEP(15)
#undef VSTEP
  }
#pragma unroll
  for (int j = 0; j < 8; ++j) acc[j] = xrow_sum(acc[j]);
  if (r == 0) {
#pragma unroll
    for (int j = 0; j < 8; ++j) red[wave * 128 + 8 * c + j] = acc[j];
  }
  __syncthreads();
  if (tid < 128) {
    float s = 0.f;
#pragma unroll
    for (int i = 0; i < NW; ++i) s += red[i * 128 + tid];
    obuf[bh * 128 + tid] = f2bf(s);
  }
}

__global__ void __launch_bounds__(256) oproj_kernel(const unsigned short* __restrict__ o, const unsigned short* __restrict__ wo, float* __restrict__ part) {
  __shared__ float red[16][8][128];
  const int ct = blockIdx.x, ks = blockIdx.y, tid = threadIdx.x, c = tid & 15, kr = tid >> 4;
  float acc[8][8];
#pragma unroll
  for (int b = 0; b < 8; ++b)
#pragma unroll
    for (int j = 0; j < 8; ++j) acc[b][j] = 0.f;
#pragma unroll
  for (int i = 0; i < 8; ++i) {
    const int k = ks * 128 + i * 16 + kr;
    const u32x4 w = *reinterpret_cast<const u32x4*>(wo + (size_t)k * D + ct * 128 + 8 * c);
#pragma unroll
    for (int b = 0; b < 8; ++b) {
      const unsigned lo = o[b * 4096 + k], hi = lo << 16;
#pragma unroll
      for (int j = 0; j < 4; ++j) { acc[b][2 * j] = dot2(w[j], lo, acc[b][2 * j]); acc[b][2 * j + 1] = dot2(w[j], hi, acc[b][2 * j + 1]); }
    }
  }
#pragma unroll
  for (int b = 0; b < 8; ++b)
#pragma unroll
    for (int j = 0; j < 8; ++j) red[kr][b][8 * c + j] = acc[b][j];
  __syncthreads();
  for (int e = tid; e < 8 * 128; e += 256) {
    const int b = e >> 7, n = e & 127;
    float s = 0.f;
    for (int i = 0; i < 16; ++i) s += red[i][b][n];
    part[((size_t)ks * 8 + b) * D + ct * 128 + n] = s;
  }
}

__global__ void __launch_bounds__(512) gateup_kernel(const unsigned short* __restrict__ x, const float* __restrict__ part,
                                                     const unsigned short* __restrict__ wg, const unsigned short* __restrict__ wu,
                                                     unsigned short* __restrict__ hbuf, unsigned short* __restrict__ act) {
  __shared__ unsigned hn[8][640];
  __shared__ float red[8][2][8][8];
  const int tid = threadIdx.x, lane = tid & 63, wave = tid >> 6;
  {
    const int b = wave;
    float v[10];
#pragma unroll
    for (int i = 0; i < 10; ++i) {
      const int d = lane + 64 * i;
      float s = 0.f;
      for (int ks = 0; ks < 32; ++ks) s += part[((size_t)ks * 8 + b) * D + d];
      v[i] = rbf(bf2f(x[b * D + d]) + rbf(s));
      if (blockIdx.x == 0) hbuf[b * D + d] = f2bf(v[i]);
    }
    ln_row(v);
#pragma unroll
    for (int i = 0; i < 10; ++i) hn[b][lane + 64 * i] = f2bf(v[i]);
  }
  __syncthreads();
  const int mat = tid & 1;
  const unsigned short* W = mat ? wu : wg;
  float acc[8][8];
#pragma unroll
  for (int b = 0; b < 8; ++b)
#pragma unroll
    for (int j = 0; j < 8; ++j) acc[b][j] = 0.f;
  for (int k = tid >> 1; k < D; k += 256) {
    const u32x4 w = *reinterpret_cast<const u32x4*>(W + (size_t)k * F + blockIdx.x * 8);
#pragma unroll
    for (int b = 0; b < 8; ++b) {
      const unsigned lo = hn[b][k], hi = lo << 16;
#pragma unroll
      for (int j = 0; j < 4; ++j) { acc[b][2 * j] = dot2(w[j], lo, acc[b][2 * j]); acc[b][2 * j + 1] = dot2(w[j], hi, acc[b][2 * j + 1]); }
    }
  }
#pragma unroll
  for (int b = 0; b < 8; ++b)
#pragma unroll
    for (int j = 0; j < 8; ++j) {
      float s = acc[b][j];
      for (int o = 2; o < 64; o <<= 1) s += __shfl_xor(s, o);
      if (lane < 2) red[wave][lane][b][j] = s;
    }
  __syncthreads();
  if (tid < 64) {
    const int b = tid >> 3, j = tid & 7;
    float g = 0.f, u = 0.f;
    for (int w = 0; w < 8; ++w) { g += red[w][0][b][j]; u += red[w][1][b][j]; }
    g = rbf(g); u = rbf(u);
    const float sg = rbf(g / (1.f + __expf(-g)));
    act[b * F + blockIdx.x * 8 + j] = f2bf(sg * u);
  }
}

__global__ void __launch_bounds__(256) down_kernel(const unsigned short* __restrict__ hbuf, const unsigned short* __restrict__ act,
                                                   const unsigned short* __restrict__ wd, unsigned short* __restrict__ out) {
  __shared__ float red[4][8][8];
  const int tid = threadIdx.x, lane = tid & 63, wave = tid >> 6;
  float acc[8][8];
#pragma unroll
  for (int b = 0; b < 8; ++b)
#pragma unroll
    for (int j = 0; j < 8; ++j) acc[b][j] = 0.f;
  for (int k = tid; k < F; k += 256) {
    const u32x4 w = *reinterpret_cast<const u32x4*>(wd + (size_t)k * D + blockIdx.x * 8);
#pragma unroll
    for (int b = 0; b < 8; ++b) {
      const unsigned lo = act[b * F + k], hi = lo << 16;
#pragma unroll
      for (int j = 0; j < 4; ++j) { acc[b][2 * j] = dot2(w[j], lo, acc[b][2 * j]); acc[b][2 * j + 1] = dot2(w[j], hi, acc[b][2 * j + 1]); }
    }
  }
#pragma unroll
  for (int b = 0; b < 8; ++b)
#pragma unroll
    for (int j = 0; j < 8; ++j) {
      float s = acc[b][j];
      for (int o = 1; o < 64; o <<= 1) s += __shfl_xor(s, o);
      if (lane == 0) red[wave][b][j] = s;
    }
  __syncthreads();
  if (tid < 64) {
    const int b = tid >> 3, j = tid & 7;
    float s = red[0][b][j] + red[1][b][j] + red[2][b][j] + red[3][b][j];
    const int d = blockIdx.x * 8 + j;
    out[b * D + d] = f2bf(bf2f(hbuf[b * D + d]) + rbf(s));
  }
}

extern "C" void kernel_launch(void* const* d_in, const int* in_sizes, int n_in, void* d_out, int out_size, void* d_ws, size_t ws_size,
                              hipStream_t stream) {
  const unsigned short* x = (const unsigned short*)d_in[0];
  const unsigned short* kc = (const unsigned short*)d_in[1];
  const unsigned short* vc = (const unsigned short*)d_in[2];
  const unsigned short* wq = (const unsigned short*)d_in[3];
  const unsigned short* wo = (const unsigned short*)d_in[4];
  const unsigned short* wg = (const unsigned short*)d_in[6];
  const unsigned short* wu = (const unsigned short*)d_in[7];
  const unsigned short* wd = (const unsigned short*)d_in[8];
  char* ws = (char*)d_ws;
  unsigned short* obuf = (unsigned short*)ws;
  float* part = (float*)(ws + 65536);
  unsigned short* hbuf = (unsigned short*)(ws + 65536 + 655360);
  unsigned short* act = (unsigned short*)(ws + 65536 + 655360 + 16384);
  attn_kernel<<<256, NW * 64, 0, stream>>>(x, kc, vc, wq, obuf);
  oproj_kernel<<<dim3(5, 32), 256, 0, stream>>>(obuf, wo, part);
  gateup_kernel<<<216, 512, 0, stream>>>(x, part, wg, wu, hbuf, act);
  down_kernel<<<80, 256, 0, stream>>>(hbuf, act, wd, (unsigned short*)d_out);
}
```

```cpp
#include <hip/hip_runtime.h>
#include <cstdint>

#define DIST 10
#define PACE 40
#define WSKIP 0
#define N1 0
#define ROT 1
#define PSLEEP 2
#define QAUX 2
#define WAUX 2

namespace {
constexpr int B = 8, D = 640, H = 32, SK = 4096, DH = 128, F = 1728, NF = 27;
using u32x4 = __attribute__((ext_vector_type(4))) unsigned;
using u32x2 = __attribute__((ext_vector_type(2))) unsigned;
using f32x4 = __attribute__((ext_vector_type(4))) float;
using f32x2 = __attribute__((ext_vector_type(2))) float;
using bf16x2 = __attribute__((ext_vector_type(2))) __bf16;
using u64 = unsigned long long;
typedef __attribute__((address_space(1))) u64 gu64;
typedef __amdgpu_buffer_rsrc_t rsrc_t;

constexpr int WS_EPOCH = 0;
constexpr int WS_READY = 4096;
constexpr int WS_GH = 8192;
constexpr int WS_R = 65536;
constexpr int WS_BYTES = WS_R + 256 * 10240;
constexpr int WS_DIAG = 4 << 20;
constexpr int L_GATE = 0, L_SC = 81920, L_PART = 98304, L_OP = 103424, L_ML = 104448, L_ST = 104464, L_ACT = 104544, L_CNT = 104672, L_END = 104688;
constexpr unsigned TAG = 1u;
constexpr int SPIN_CAP = 1 << 16;

__device__ __forceinline__ float dot2(unsigned a, unsigned b, float acc) {
  return __builtin_amdgcn_fdot2_f32_bf16(__builtin_bit_cast(bf16x2, a), __builtin_bit_cast(bf16x2, b), acc, false);
}
__device__ __forceinline__ float asf(unsigned u) { return __builtin_bit_cast(float, u); }
__device__ __forceinline__ unsigned asu(float f) { return __builtin_bit_cast(unsigned, f); }
__device__ __forceinline__ unsigned f2bf(float f) { return __builtin_bit_cast(unsigned short, (__bf16)f); }
__device__ __forceinline__ float rbf(float f) { return asf(f2bf(f) << 16); }
__device__ __forceinline__ unsigned pkbf(float lo, float hi) { return __builtin_bit_cast(unsigned, bf16x2{(__bf16)lo, (__bf16)hi}); }
template <int CTRL> __device__ __forceinline__ unsigned dppu(unsigned x) { return (unsigned)__builtin_amdgcn_mov_dpp((int)x, CTRL, 0xf, 0xf, true); }
template <int CTRL> __device__ __forceinline__ float dpp(float x) { return asf(dppu<CTRL>(asu(x))); }
constexpr int XOR1 = 0xB1, XOR2 = 0x4E, XOR7 = 0x141, ROR8 = 0x128, ROR4 = 0x124, BC = 0x150;
__device__ __forceinline__ float fold32(float a, float b) {
  auto s = __builtin_amdgcn_permlane32_swap(asu(a), asu(b), false, false);
  return asf(s[0]) + asf(s[1]);
}
__device__ __forceinline__ float fold16(float a, float b) {
  auto s = __builtin_amdgcn_permlane16_swap(asu(a), asu(b), false, false);
  return asf(s[0]) + asf(s[1]);
}
__device__ __forceinline__ float wave_sum(float x) {
  x += dpp<XOR1>(x); x += dpp<XOR2>(x); x += dpp<XOR7>(x); x += dpp<ROR8>(x);
  x = fold16(x, x);
  return fold32(x, x);
}
__device__ __forceinline__ float wave_max(float x) {
  x = fmaxf(x, dpp<XOR1>(x)); x = fmaxf(x, dpp<XOR2>(x)); x = fmaxf(x, dpp<XOR7>(x)); x = fmaxf(x, dpp<ROR8>(x));
  auto s = __builtin_amdgcn_permlane16_swap(asu(x), asu(x), false, false);
  x = fmaxf(asf(s[0]), asf(s[1]));
  auto t = __builtin_amdgcn_permlane32_swap(asu(x), asu(x), false, false);
  return fmaxf(asf(t[0]), asf(t[1]));
}
__device__ __forceinline__ f32x2 fold8to2(const float (&a)[8]) {
  const float t0 = fold32(a[0], a[4]), t1 = fold32(a[1], a[5]), t2 = fold32(a[2], a[6]), t3 = fold32(a[3], a[7]);
  return f32x2{fold16(t0, t2), fold16(t1, t3)};
}
__device__ __forceinline__ float fold8to1(const float (&a)[8], int lane) {
  const f32x2 u = fold8to2(a);
  const float s0 = u[0] + dpp<ROR8>(u[0]), s1 = u[1] + dpp<ROR8>(u[1]);
  return (lane & 8) ? s1 : s0;
}
template <int K> __device__ __forceinline__ unsigned bcast8(unsigned x) { return (unsigned)__builtin_amdgcn_ds_swizzle((int)x, (K << 5) | 0x18); }
__device__ __forceinline__ rsrc_t mkrsrc(const void* p, unsigned bytes) { return __builtin_amdgcn_make_buffer_rsrc((void*)p, 0, bytes, 0x00020000); }
__device__ __forceinline__ u64 ld_agent(const char* p) { return __hip_atomic_load((gu64*)p, __ATOMIC_RELAXED, __HIP_MEMORY_SCOPE_AGENT); }
__device__ __forceinline__ void st_agent(char* p, u64 v) { __hip_atomic_store((gu64*)p, v, __ATOMIC_RELAXED, __HIP_MEMORY_SCOPE_AGENT); }
__device__ __forceinline__ void st_gran(char* p, float v) { st_agent(p, ((u64)TAG << 32) | asu(v)); }
#define SB() __builtin_amdgcn_sched_barrier(0)
#define BARRIER() asm volatile("s_waitcnt lgkmcnt(0)\n\ts_barrier" ::: "memory")
#define PIN(x) asm volatile("" : "+v"(x))
#define STAMP(i)
#define DUMP()

template <int NP> __device__ __forceinline__ void interleave(const u32x4 (&w)[2 * NP], unsigned (&p)[NP][8]) {
#pragma unroll
  for (int k = 0; k < NP; ++k)
#pragma unroll
    for (int j = 0; j < 4; ++j) {
      p[k][2 * j] = __builtin_amdgcn_perm(w[2 * k + 1][j], w[2 * k][j], 0x05040100u);
      p[k][2 * j + 1] = __builtin_amdgcn_perm(w[2 * k + 1][j], w[2 * k][j], 0x07060302u);
    }
}

__device__ __forceinline__ float gather32(const char* g, bool need0, bool need1) {
  u64 v0 = (u64)TAG << 32, v1 = (u64)TAG << 32;
#pragma unroll 1
  for (int spin = 0; spin < SPIN_CAP; ++spin) {
    if (need0) v0 = ld_agent(g);
    if (need1) v1 = ld_agent(g + 512);
    need0 = (unsigned)(v0 >> 32) != TAG; need1 = (unsigned)(v1 >> 32) != TAG;
    if (!__any(need0 || need1)) break;
    __builtin_amdgcn_s_sleep(PSLEEP);
  }
  float s = asf((unsigned)v0) + asf((unsigned)v1);
  s += dpp<ROR8>(s); s += dpp<ROR4>(s);
  s = fold16(s, s);
  return fold32(s, s);
}

enum { K_FIRST, K_EXT, K_RAMP, K_LOOP, K_LAST };
template <int MODE>
__device__ __forceinline__ float kbatch(u32x4 (&buf)[16], const u32x4 (&ext)[16], const u32x4 q, rsrc_t krs, rsrc_t vrs, int voff, int soff, int c) {
  float xx[16];
#pragma unroll
  for (int i = 0; i < 16; ++i) {
    const u32x4 k = MODE == K_EXT ? ext[i] : buf[i];
    float d0 = dot2(k[0], q[0], 0.f), d1 = dot2(k[1], q[1], 0.f);
    d0 = dot2(k[2], q[2], d0); d1 = dot2(k[3], q[3], d1);
    xx[i] = d0 + d1;
    PIN(xx[i]);
    SB();
    if (MODE == K_FIRST) buf[i] = __builtin_amdgcn_raw_buffer_load_b128(krs, voff, (32 + i) * 2048, 2);
    if (MODE == K_LOOP || (MODE == K_RAMP && i + DIST >= 16) || (MODE == K_LAST && i + DIST < 16))
      buf[(i + DIST) & 15] = __builtin_amdgcn_raw_buffer_load_b128(krs, voff, soff + (i + DIST) * 2048, 2);
    if (MODE == K_LAST && i + DIST >= 16) buf[(i + DIST) & 15] = __builtin_amdgcn_raw_buffer_load_b128(vrs, voff, (i + DIST - 16) * 2048, 2);
    SB();
  }
#define TR_STEP(HALF, CTRL, BIT) _Pragma("unroll") for (int hh = 0; hh < (HALF); ++hh) { \
    const float lo_ = xx[hh], hi_ = xx[hh + (HALF)]; const float t1_ = lo_ + dpp<CTRL>(lo_), t2_ = hi_ + dpp<CTRL>(hi_); \
    xx[hh] = (c & (BIT)) ? t2_ : t1_; }
  TR_STEP(8, ROR8, 8) TR_STEP(4, XOR7, 4) TR_STEP(2, XOR2, 2) TR_STEP(1, XOR1, 1)
#undef TR_STEP
  return rbf(rbf(xx[0]) * 0.08838834764831845f);
}
template <int I, bool LAST>
__device__ __forceinline__ void vstep(u32x4 (&buf)[16], float (&acc)[8], unsigned pl, rsrc_t vrs, int voff, int soff) {
  const u32x4 v = buf[I];
  const unsigned lo = dppu<BC + I>(pl), hi = lo << 16;
#pragma unroll
  for (int j = 0; j < 4; ++j) { acc[2 * j] = dot2(v[j], lo, acc[2 * j]); acc[2 * j + 1] = dot2(v[j], hi, acc[2 * j + 1]); }
#pragma unroll
  for (int j = 0; j < 8; ++j) PIN(acc[j]);
  SB();
  if (!LAST || I + DIST < 16) buf[(I + DIST) & 15] = __builtin_amdgcn_raw_buffer_load_b128(vrs, voff, soff + (I + DIST) * 2048, 2);
  SB();
}
template <bool LAST>
__device__ __forceinline__ void vbatch(u32x4 (&buf)[16], float (&acc)[8], unsigned pl, rsrc_t vrs, int voff, int soff) {
#define VS(i) vstep<i, LAST>(buf, acc, pl, vrs, voff, soff);
  VS(0) VS(1) VS(2) VS(3) VS(4) VS(5) VS(6) VS(7) VS(8) VS(9) VS(10) VS(11) VS(12) VS(13) VS(14) VS(15)
#undef VS
}

__global__ void __launch_bounds__(768, 3)
fused_layer(const unsigned short* __restrict__ x, const unsigned short* __restrict__ kc, const unsigned short* __restrict__ vc,
            const unsigned short* __restrict__ wq, const unsigned short* __restrict__ wo, const unsigned short* __restrict__ wg,
            const unsigned short* __restrict__ wu, const unsigned short* __restrict__ wd, unsigned short* __restrict__ out, char* ws) {
  __shared__ __attribute__((aligned(16))) char lds[L_END];
  const int bh = blockIdx.x, b = bh >> 5, h = bh & 31;
  const int tid = threadIdx.x, lane = tid & 63, wave = __builtin_amdgcn_readfirstlane(tid >> 6);
  STAMP(0);
  if (wave < 2) {
    __builtin_amdgcn_s_setprio(2);
    const int r = lane >> 4, c = lane & 15;
    const rsrc_t krs = mkrsrc(kc + (size_t)bh * SK * DH, SK * DH * 2), vrs = mkrsrc(vc + (size_t)bh * SK * DH, SK * DH * 2);
    const int voff = tid * 16;
    u32x4 buf[16], ext[16];
#pragma unroll
    for (int i = 0; i < 16; ++i) {
      if (i == N1) BARRIER();
      buf[i] = __builtin_amdgcn_raw_buffer_load_b128(krs, voff, i * 2048, 2); SB();
    }
#pragma unroll
    for (int i = 0; i < 16; ++i) { ext[i] = __builtin_amdgcn_raw_buffer_load_b128(krs, voff, (16 + i) * 2048, 2); SB(); }
    STAMP(4);
    BARRIER();
    u32x4 q;
    {
      f32x2 s = {0.f, 0.f};
#pragma unroll
      for (int i = 0; i < 10; ++i) s += *(const f32x2*)(lds + L_PART + i * 512 + (8 * c + 2 * r) * 4);
      const unsigned v = pkbf(s[0], s[1]);
      auto a = __builtin_amdgcn_permlane16_swap(v, v, false, false);
      auto e = __builtin_amdgcn_permlane32_swap(a[0], a[0], false, false);
      auto o = __builtin_amdgcn_permlane32_swap(a[1], a[1], false, false);
      q = u32x4{e[0], o[0], e[1], o[1]};
    }
    STAMP(1);
    float* sc = (float*)(lds + L_SC) + wave * 2048 + lane;
    float m, s;
    m = s = kbatch<K_FIRST>(buf, ext, q, krs, vrs, voff, 0, c); sc[0] = s;
    s = kbatch<K_EXT>(buf, ext, q, krs, vrs, voff, 0, c); sc[64] = s; m = fmaxf(m, s);
    s = kbatch<K_RAMP>(buf, ext, q, krs, vrs, voff, 2 * 32768, c); sc[128] = s; m = fmaxf(m, s);
#pragma unroll 1
    for (int bt = 3; bt < 31; ++bt) { s = kbatch<K_LOOP>(buf, ext, q, krs, vrs, voff, bt * 32768, c); sc[bt * 64] = s; m = fmaxf(m, s); }
    s = kbatch<K_LAST>(buf, ext, q, krs, vrs, voff, 31 * 32768, c); sc[31 * 64] = s; m = fmaxf(m, s);
    s = sc[0];
    m = wave_max(m);
    STAMP(2);
    float acc[8], l = 0.f;
#pragma unroll
    for (int j = 0; j < 8; ++j) acc[j] = 0.f;
#pragma unroll 1
    for (int bt = 0; bt < 31; ++bt) {
      const float e = __expf(s - m);
      s = sc[bt * 64 + 64];
      l += e;
      vbatch<false>(buf, acc, f2bf(e), vrs, voff, bt * 32768);
    }
    {
      const float e = __expf(s - m);
      l += e;
      l = wave_sum(l);
      vbatch<true>(buf, acc, f2bf(e), vrs, voff, 31 * 32768);
    }
    *(f32x2*)(lds + L_OP + wave * 512 + (8 * c + 2 * r) * 4) = fold8to2(acc);
    if (lane == 0) *(f32x2*)(lds + L_ML + wave * 8) = f32x2{m, l};
    STAMP(3);
    BARRIER();
    DUMP();
    return;
  }
  const int w = wave - 2, t = tid - 128;
  const int rg = lane >> 3, cc = lane & 7;
  const bool ffn = h < NF;
  float xres;
  {
    const int r = lane >> 4, c = lane & 15;
    const unsigned* x32 = (const unsigned*)(x + b * D);
    unsigned xa[5];
#pragma unroll
    for (int i = 0; i < 5; ++i) xa[i] = x32[lane + 64 * i];
    const int rot = ROT ? 2 * b : 0;
    const float xo = asf((unsigned)x[b * D + 64 * w + 16 * r + ((c + rot) & 15)] << 16);
    xres = asf((unsigned)x[b * D + 20 * h + 4 * (w < 5 ? w : 0) + (lane & 3)] << 16);
    SB();
    const rsrc_t qrs = mkrsrc(wq, D * H * DH * 2);
    u32x4 wv[16];
#pragma unroll
    for (int i = 0; i < 16; ++i) { wv[i] = (WSKIP & 8) ? u32x4{xa[0], xa[1], xa[2], xa[3]} : __builtin_amdgcn_raw_buffer_load_b128(qrs, (64 * w + 16 * r) * 8192 + h * 256 + c * 16, ((i + rot) & 15) * 8192, QAUX); SB(); }
    BARRIER();
    if (t == 0) *(unsigned*)(lds + L_CNT) = 0;
    float s = 0.f;
#pragma unroll
    for (int i = 0; i < 5; ++i) s += asf(xa[i] << 16) + asf(xa[i] & 0xffff0000u);
    const float mean = wave_sum(s) * (1.f / D);
    STAMP(9);
    float ss = 0.f;
#pragma unroll
    for (int i = 0; i < 5; ++i) { const float d0 = asf(xa[i] << 16) - mean, d1 = asf(xa[i] & 0xffff0000u) - mean; ss += d0 * d0 + d1 * d1; }
    const float var = wave_sum(ss) * (1.f / D);
    const float sd = rbf(sqrtf(rbf(rbf(var) + 1e-5f)));
    const unsigned xl = f2bf(rbf(xo - rbf(mean)) / sd), xh = xl << 16;
    STAMP(10);
    float acc[8];
#pragma unroll
    for (int j = 0; j < 8; ++j) acc[j] = 0.f;
#define QS(i) { const unsigned lo = dppu<BC + i>(xl), hi = dppu<BC + i>(xh); \
    _Pragma("unroll") for (int j = 0; j < 4; ++j) { acc[2 * j] = dot2(wv[i][j], lo, acc[2 * j]); acc[2 * j + 1] = dot2(wv[i][j], hi, acc[2 * j + 1]); } }
    QS(0) QS(1) QS(2) QS(3) QS(4) QS(5) QS(6) QS(7) QS(8) QS(9) QS(10) QS(11) QS(12) QS(13) QS(14) QS(15)
#undef QS
    *(f32x2*)(lds + L_PART + w * 512 + (8 * c + 2 * r) * 4) = fold8to2(acc);
  }
  STAMP(11);
  BARRIER();
  STAMP(1);
  {
    const rsrc_t wsr = mkrsrc(ws, WS_BYTES);
    const u32x4 z = {0u, 0u, 0u, 0u};
    __builtin_amdgcn_raw_buffer_store_b128(z, wsr, WS_R + bh * 10240 + t * 16, 0, 16);
    if (t < 10) __builtin_amdgcn_raw_buffer_store_b128(z, wsr, WS_GH + b * 5120 + h * 160 + t * 16, 0, 16);
  }
  const u64 token = ld_agent(ws + WS_EPOCH + b * 256) * 6364136223846793005ull + 1442695040888963407ull;
  unsigned po[8][8], pu[4][8], pd[4][8];
  if (ffn) {
    u32x4 wv[8];
    const int voff = (64 * w + 8 * rg) * (F * 2) + h * 128 + cc * 16;
    {
      const rsrc_t rs = mkrsrc(wg, D * F * 2);
#pragma unroll
      for (int i = 0; i < 8; ++i) { __builtin_amdgcn_s_sleep(PACE); wv[i] = (WSKIP & 1) ? u32x4{0u, 0u, 0u, 0u} : __builtin_amdgcn_raw_buffer_load_b128(rs, voff, i * (F * 2), WAUX); SB(); }
      unsigned pg[4][8];
      interleave<4>(wv, pg);
#pragma unroll
      for (int k = 0; k < 4; ++k) {
        *(u32x4*)(lds + L_GATE + (w * 8 + 2 * k) * 1024 + lane * 16) = u32x4{pg[k][0], pg[k][1], pg[k][2], pg[k][3]};
        *(u32x4*)(lds + L_GATE + (w * 8 + 2 * k + 1) * 1024 + lane * 16) = u32x4{pg[k][4], pg[k][5], pg[k][6], pg[k][7]};
      }
      SB();
    }
    {
      const rsrc_t rs = mkrsrc(wu, D * F * 2);
#pragma unroll
      for (int i = 0; i < 8; ++i) { __builtin_amdgcn_s_sleep(PACE); wv[i] = (WSKIP & 1) ? u32x4{0u, 0u, 0u, 0u} : __builtin_amdgcn_raw_buffer_load_b128(rs, voff, i * (F * 2), WAUX); SB(); }
      interleave<4>(wv, pu);
#pragma unroll
      for (int k = 0; k < 4; ++k)
#pragma unroll
        for (int j = 0; j < 8; ++j) PIN(pu[k][j]);
      SB();
    }
    {
      const rsrc_t rs = mkrsrc(wd, D * F * 2);
#pragma unroll
      for (int i = 0; i < 8; ++i) { __builtin_amdgcn_s_sleep(PACE); wv[i] = (WSKIP & 2) ? u32x4{0u, 0u, 0u, 0u} : __builtin_amdgcn_raw_buffer_load_b128(rs, (64 * h + 8 * rg) * (D * 2) + w * 128 + cc * 16, i * (D * 2), WAUX); SB(); }
      interleave<4>(wv, pd);
#pragma unroll
      for (int k = 0; k < 4; ++k)
#pragma unroll
        for (int j = 0; j < 8; ++j) PIN(pd[k][j]);
      SB();
    }
  } else {
#pragma unroll
    for (int k = 0; k < 4; ++k)
#pragma unroll
      for (int j = 0; j < 8; ++j) pu[k][j] = pd[k][j] = 0u;
  }
  {
    const rsrc_t rs = mkrsrc(wo, H * DH * D * 2);
#pragma unroll
    for (int hf = 0; hf < 2; ++hf) {
      u32x4 wv[8];
#pragma unroll
      for (int i = 0; i < 8; ++i) { __builtin_amdgcn_s_sleep(PACE); wv[i] = (WSKIP & 4) ? u32x4{0u, 0u, 0u, 0u} : __builtin_amdgcn_raw_buffer_load_b128(rs, (128 * h + 16 * rg) * (D * 2) + w * 128 + cc * 16, (8 * hf + i) * (D * 2), WAUX); SB(); }
      unsigned pt[4][8];
      interleave<4>(wv, pt);
#pragma unroll
      for (int k = 0; k < 4; ++k)
#pragma unroll
        for (int j = 0; j < 8; ++j) { po[4 * hf + k][j] = pt[k][j]; PIN(po[4 * hf + k][j]); }
      SB();
    }
  }
  asm volatile("s_waitcnt vmcnt(0)" ::: "memory");
  if (lane == 0) {
    const unsigned old = __hip_atomic_fetch_add((unsigned*)(lds + L_CNT), 1u, __ATOMIC_RELAXED, __HIP_MEMORY_SCOPE_WORKGROUP);
    if (old == 9) st_agent(ws + WS_READY + bh * 8, token);
  }
  if (w == 0) {
  #pragma unroll 1
  for (int spin = 0; spin < SPIN_CAP; ++spin) {
      const u64 v = ld_agent(ws + WS_READY + (b * 32 + (lane & 31)) * 8);
      if (__all(v == token)) break;
      __builtin_amdgcn_s_sleep(16);
    }
    if (h == 0 && lane == 0) st_agent(ws + WS_EPOCH + b * 256, token);
  }
  const int col = 64 * w + 8 * cc + rg, jc = col / 20, rem = col - 20 * jc;
  char* const pub = ws + WS_R + (b * 32 + jc) * 10240 + ((rem >> 2) * 128 + ((h & 7) * 4 + (h >> 3)) * 4 + (rem & 3)) * 8;
  const char* const own = ws + WS_R + bh * 10240 + (w * 128 + lane) * 8;
  STAMP(2);
  BARRIER();
  STAMP(3);
  {
    const f32x2 o0 = *(const f32x2*)(lds + L_OP + lane * 8), o1 = *(const f32x2*)(lds + L_OP + 512 + lane * 8);
    const f32x4 ml = *(const f32x4*)(lds + L_ML);
    const float M = fmaxf(ml[0], ml[2]), f0 = __expf(ml[0] - M), f1 = __expf(ml[2] - M);
    const float inv = 1.f / (ml[1] * f0 + ml[3] * f1), s0 = f0 * inv, s1 = f1 * inv;
    const unsigned pk = pkbf(o0[0] * s0 + o1[0] * s1, o0[1] * s0 + o1[1] * s1);
    const unsigned ok[8] = {bcast8<0>(pk), bcast8<1>(pk), bcast8<2>(pk), bcast8<3>(pk), bcast8<4>(pk), bcast8<5>(pk), bcast8<6>(pk), bcast8<7>(pk)};
    float acc[8];
#pragma unroll
    for (int j = 0; j < 8; ++j) acc[j] = 0.f;
#pragma unroll
    for (int k = 0; k < 8; ++k)
#pragma unroll
      for (int j = 0; j < 8; ++j) acc[j] = dot2(po[k][j], ok[k], acc[j]);
    st_gran(pub, fold8to1(acc, lane));
  }
  STAMP(4);
  u32x4 pg[8];
  if (ffn) {
#pragma unroll
    for (int k = 0; k < 8; ++k) pg[k] = *(const u32x4*)(lds + L_GATE + (w * 8 + k) * 1024 + lane * 16);
  } else if (w >= 5) { DUMP(); return; }
  float hres = 0.f;
  if (w < 5) {
    hres = rbf(xres + rbf(gather32(own, true, true)));
    if (lane < 4) st_gran(ws + WS_GH + (b * 640 + 20 * h + 4 * w + lane) * 8, hres);
  }
  STAMP(5);
  if (ffn) {
    BARRIER();
    u64 g;
  #pragma unroll 1
  for (int spin = 0; spin < SPIN_CAP; ++spin) {
      g = ld_agent(ws + WS_GH + (b * 640 + t) * 8);
      if (__all((unsigned)(g >> 32) == TAG)) break;
    }
    STAMP(6);
    const float hv = asf((unsigned)g);
    const float mw = wave_sum(hv) * (1.f / 64), dv = hv - mw, m2 = wave_sum(dv * dv);
    if (lane == 0) *(f32x2*)(lds + L_ST + w * 8) = f32x2{mw, m2};
    BARRIER();
    f32x4 st[5];
#pragma unroll
    for (int i = 0; i < 5; ++i) st[i] = *(const f32x4*)(lds + L_ST + i * 16);
    float mean = 0.f, s2 = 0.f, sb = 0.f;
#pragma unroll
    for (int i = 0; i < 5; ++i) { mean += st[i][0] + st[i][2]; s2 += st[i][1] + st[i][3]; }
    mean *= 0.1f;
#pragma unroll
    for (int i = 0; i < 5; ++i) { const float d0 = st[i][0] - mean, d1 = st[i][2] - mean; sb += d0 * d0 + d1 * d1; }
    const float var = (s2 + 64.f * sb) * (1.f / D);
    const float sd = rbf(sqrtf(rbf(rbf(var) + 1e-5f)));
    const unsigned hl = f2bf(rbf(hv - rbf(mean)) / sd);
    const unsigned pk = hl | (dppu<XOR1>(hl) << 16);
    const unsigned hk[4] = {bcast8<0>(pk), bcast8<2>(pk), bcast8<4>(pk), bcast8<6>(pk)};
    float ag[8], au[8];
#pragma unroll
    for (int j = 0; j < 8; ++j) ag[j] = au[j] = 0.f;
#pragma unroll
    for (int k = 0; k < 4; ++k)
#pragma unroll
      for (int j = 0; j < 8; ++j) { ag[j] = dot2(pg[2 * k + (j >> 2)][j & 3], hk[k], ag[j]); au[j] = dot2(pu[k][j], hk[k], au[j]); }
    *(f32x2*)(lds + L_PART + w * 512 + (8 * cc + rg) * 8) = f32x2{fold8to1(ag, lane), fold8to1(au, lane)};
    BARRIER();
    if (w == 2) {
      f32x2 s = {0.f, 0.f};
#pragma unroll
      for (int i = 0; i < 10; ++i) s += *(const f32x2*)(lds + L_PART + i * 512 + lane * 8);
      const float gt = rbf(s[0]), up = rbf(s[1]);
      *(unsigned short*)(lds + L_ACT + lane * 2) = (unsigned short)f2bf(rbf(gt / (1.f + __expf(-gt))) * up);
    }
    BARRIER();
    const u32x4 ak = *(const u32x4*)(lds + L_ACT + rg * 16);
    float acc[8];
#pragma unroll
    for (int j = 0; j < 8; ++j) acc[j] = 0.f;
#pragma unroll
    for (int k = 0; k < 4; ++k)
#pragma unroll
      for (int j = 0; j < 8; ++j) acc[j] = dot2(pd[k][j], ak[k], acc[j]);
    st_gran(pub + 5120, fold8to1(acc, lane));
    STAMP(7);
    if (w >= 5) { DUMP(); return; }
  }
  {
    const int s0 = lane >> 2, s1 = s0 + 16;
    const float s = gather32(own + 5120, (s0 >> 2) + 8 * (s0 & 3) < NF, (s1 >> 2) + 8 * (s1 & 3) < NF);
    if (lane < 4) out[b * D + 20 * h + 4 * w + lane] = (unsigned short)f2bf(hres + rbf(s));
  }
  STAMP(8);
  DUMP();
}
}

extern "C" void kernel_launch(void* const* d_in, const int* in_sizes, int n_in, void* d_out, int out_size, void* d_ws, size_t ws_size,
                              hipStream_t stream) {
  fused_layer<<<256, 768, 0, stream>>>((const unsigned short*)d_in[0], (const unsigned short*)d_in[1], (const unsigned short*)d_in[2],
                                       (const unsigned short*)d_in[3], (const unsigned short*)d_in[4], (const unsigned short*)d_in[6],
                                       (const unsigned short*)d_in[7], (const unsigned short*)d_in[8], (unsigned short*)d_out, (char*)d_ws);
}
```

```cpp
#include <hip/hip_runtime.h>
#include <cstdint>

#define DIST 11
#define PACE 40
#define WSKIP 0
#define N1 0
#define ROT 1
#define PSLEEP 2
#define QAUX 2
#define KAUX 18
#define VAUX 18
#define WAUX 2

namespace {
constexpr int B = 8, D = 640, H = 32, SK = 4096, DH = 128, F = 1728, NF = 27;
using u32x4 = __attribute__((ext_vector_type(4))) unsigned;
using u32x2 = __attribute__((ext_vector_type(2))) unsigned;
using f32x4 = __attribute__((ext_vector_type(4))) float;
using f32x2 = __attribute__((ext_vector_type(2))) float;
using bf16x2 = __attribute__((ext_vector_type(2))) __bf16;
using u64 = unsigned long long;
typedef __attribute__((address_space(1))) u64 gu64;
typedef __amdgpu_buffer_rsrc_t rsrc_t;

constexpr int WS_EPOCH = 0;
constexpr int WS_READY = 4096;
constexpr int WS_GH = 8192;
constexpr int WS_R = 65536;
constexpr int WS_BYTES = WS_R + 256 * 10240;
constexpr int WS_DIAG = 4 << 20;
constexpr int L_GATE = 0, L_SC = 81920, L_PART = 98304, L_OP = 103424, L_ML = 104448, L_ST = 104464, L_ACT = 104544, L_CNT = 104672, L_END = 104688;
constexpr unsigned TAG = 1u;
constexpr int SPIN_CAP = 1 << 16;

__device__ __forceinline__ float dot2(unsigned a, unsigned b, float acc) {
  return __builtin_amdgcn_fdot2_f32_bf16(__builtin_bit_cast(bf16x2, a), __builtin_bit_cast(bf16x2, b), acc, false);
}
__device__ __forceinline__ float asf(unsigned u) { return __builtin_bit_cast(float, u); }
__device__ __forceinline__ unsigned asu(float f) { return __builtin_bit_cast(unsigned, f); }
__device__ __forceinline__ unsigned f2bf(float f) { return __builtin_bit_cast(unsigned short, (__bf16)f); }
__device__ __forceinline__ float rbf(float f) { return asf(f2bf(f) << 16); }
__device__ __forceinline__ unsigned pkbf(float lo, float hi) { return __builtin_bit_cast(unsigned, bf16x2{(__bf16)lo, (__bf16)hi}); }
template <int CTRL> __device__ __forceinline__ unsigned dppu(unsigned x) { return (unsigned)__builtin_amdgcn_mov_dpp((int)x, CTRL, 0xf, 0xf, true); }
template <int CTRL> __device__ __forceinline__ float dpp(float x) { return asf(dppu<CTRL>(asu(x))); }
constexpr int XOR1 = 0xB1, XOR2 = 0x4E, XOR7 = 0x141, ROR8 = 0x128, ROR4 = 0x124, BC = 0x150;
__device__ __forceinline__ float fold32(float a, float b) {
  auto s = __builtin_amdgcn_permlane32_swap(asu(a), asu(b), false, false);
  return asf(s[0]) + asf(s[1]);
}
__device__ __forceinline__ float fold16(float a, float b) {
  auto s = __builtin_amdgcn_permlane16_swap(asu(a), asu(b), false, false);
  return asf(s[0]) + asf(s[1]);
}
__device__ __forceinline__ float wave_sum(float x) {
  x += dpp<XOR1>(x); x += dpp<XOR2>(x); x += dpp<XOR7>(x); x += dpp<ROR8>(x);
  x = fold16(x, x);
  return fold32(x, x);
}
__device__ __forceinline__ float wave_max(float x) {
  x = fmaxf(x, dpp<XOR1>(x)); x = fmaxf(x, dpp<XOR2>(x)); x = fmaxf(x, dpp<XOR7>(x)); x = fmaxf(x, dpp<ROR8>(x));
  auto s = __builtin_amdgcn_permlane16_swap(asu(x), asu(x), false, false);
  x = fmaxf(asf(s[0]), asf(s[1]));
  auto t = __builtin_amdgcn_permlane32_swap(asu(x), asu(x), false, false);
  return fmaxf(asf(t[0]), asf(t[1]));
}
__device__ __forceinline__ f32x2 fold8to2(const float (&a)[8]) {
  const float t0 = fold32(a[0], a[4]), t1 = fold32(a[1], a[5]), t2 = fold32(a[2], a[6]), t3 = fold32(a[3], a[7]);
  return f32x2{fold16(t0, t2), fold16(t1, t3)};
}
__device__ __forceinline__ float fold8to1(const float (&a)[8], int lane) {
  const f32x2 u = fold8to2(a);
  const float s0 = u[0] + dpp<ROR8>(u[0]), s1 = u[1] + dpp<ROR8>(u[1]);
  return (lane & 8) ? s1 : s0;
}
template <int K> __device__ __forceinline__ unsigned bcast8(unsigned x) { return (unsigned)__builtin_amdgcn_ds_swizzle((int)x, (K << 5) | 0x18); }
__device__ __forceinline__ rsrc_t mkrsrc(const void* p, unsigned bytes) { return __builtin_amdgcn_make_buffer_rsrc((void*)p, 0, bytes, 0x00020000); }
__device__ __forceinline__ u64 ld_agent(const char* p) { return __hip_atomic_load((gu64*)p, __ATOMIC_RELAXED, __HIP_MEMORY_SCOPE_AGENT); }
__device__ __forceinline__ void st_agent(char* p, u64 v) { __hip_atomic_store((gu64*)p, v, __ATOMIC_RELAXED, __HIP_MEMORY_SCOPE_AGENT); }
__device__ __forceinline__ void st_gran(char* p, float v) { st_agent(p, ((u64)TAG << 32) | asu(v)); }
#define SB() __builtin_amdgcn_sched_barrier(0)
#define BARRIER() asm volatile("s_waitcnt lgkmcnt(0)\n\ts_barrier" ::: "memory")
#define PIN(x) asm volatile("" : "+v"(x))
#define STAMP(i)
#define DUMP()

template <int NP> __device__ __forceinline__ void interleave(const u32x4 (&w)[2 * NP], unsigned (&p)[NP][8]) {
#pragma unroll
  for (int k = 0; k < NP; ++k)
#pragma unroll
    for (int j = 0; j < 4; ++j) {
      p[k][2 * j] = __builtin_amdgcn_perm(w[2 * k + 1][j], w[2 * k][j], 0x05040100u);
      p[k][2 * j + 1] = __builtin_amdgcn_perm(w[2 * k + 1][j], w[2 * k][j], 0x07060302u);
    }
}

__device__ __forceinline__ float gather32(const char* g, bool need0, bool need1) {
  u64 v0 = (u64)TAG << 32, v1 = (u64)TAG << 32;
#pragma unroll 1
  for (int spin = 0; spin < SPIN_CAP; ++spin) {
    if (need0) v0 = ld_agent(g);
    if (need1) v1 = ld_agent(g + 512);
    need0 = (unsigned)(v0 >> 32) != TAG; need1 = (unsigned)(v1 >> 32) != TAG;
    if (!__any(need0 || need1)) break;
    __builtin_amdgcn_s_sleep(PSLEEP);
  }
  float s = asf((unsigned)v0) + asf((unsigned)v1);
  s += dpp<ROR8>(s); s += dpp<ROR4>(s);
  s = fold16(s, s);
  return fold32(s, s);
}

enum { K_FIRST, K_EXT, K_RAMP, K_LOOP, K_LAST };
template <int MODE>
__device__ __forceinline__ float kbatch(u32x4 (&buf)[16], const u32x4 (&ext)[16], const u32x4 q, rsrc_t krs, rsrc_t vrs, int voff, int soff, int c) {
  float xx[16];
#pragma unroll
  for (int i = 0; i < 16; ++i) {
    const u32x4 k = MODE == K_EXT ? ext[i] : buf[i];
    float d0 = dot2(k[0], q[0], 0.f), d1 = dot2(k[1], q[1], 0.f);
    d0 = dot2(k[2], q[2], d0); d1 = dot2(k[3], q[3], d1);
    xx[i] = d0 + d1;
    PIN(xx[i]);
    SB();
    if (MODE == K_FIRST) buf[i] = __builtin_amdgcn_raw_buffer_load_b128(krs, voff, (32 + i) * 2048, KAUX);
    if (MODE == K_LOOP || (MODE == K_RAMP && i + DIST >= 16) || (MODE == K_LAST && i + DIST < 16))
      buf[(i + DIST) & 15] = __builtin_amdgcn_raw_buffer_load_b128(krs, voff, soff + (i + DIST) * 2048, KAUX);
    if (MODE == K_LAST && i + DIST >= 16) buf[(i + DIST) & 15] = __builtin_amdgcn_raw_buffer_load_b128(vrs, voff, (i + DIST - 16) * 2048, VAUX);
    SB();
  }
#define TR_STEP(HALF, CTRL, BIT) _Pragma("unroll") for (int hh = 0; hh < (HALF); ++hh) { \
    const float lo_ = xx[hh], hi_ = xx[hh + (HALF)]; const float t1_ = lo_ + dpp<CTRL>(lo_), t2_ = hi_ + dpp<CTRL>(hi_); \
    xx[hh] = (c & (BIT)) ? t2_ : t1_; }
  TR_STEP(8, ROR8, 8) TR_STEP(4, XOR7, 4) TR_STEP(2, XOR2, 2) TR_STEP(1, XOR1, 1)
#undef TR_STEP
  return rbf(rbf(xx[0]) * 0.08838834764831845f);
}
template <int I, bool LAST>
__device__ __forceinline__ void vstep(u32x4 (&buf)[16], float (&acc)[8], unsigned pl, rsrc_t vrs, int voff, int soff) {
  const u32x4 v = buf[I];
  const unsigned lo = dppu<BC + I>(pl), hi = lo << 16;
#pragma unroll
  for (int j = 0; j < 4; ++j) { acc[2 * j] = dot2(v[j], lo, acc[2 * j]); acc[2 * j + 1] = dot2(v[j], hi, acc[2 * j + 1]); }
#pragma unroll
  for (int j = 0; j < 8; ++j) PIN(acc[j]);
  SB();
  if (!LAST || I + DIST < 16) buf[(I + DIST) & 15] = __builtin_amdgcn_raw_buffer_load_b128(vrs, voff, soff + (I + DIST) * 2048, VAUX);
  SB();
}
template <bool LAST>
__device__ __forceinline__ void vbatch(u32x4 (&buf)[16], float (&acc)[8], unsigned pl, rsrc_t vrs, int voff, int soff) {
#define VS(i) vstep<i, LAST>(buf, acc, pl, vrs, voff, soff);
  VS(0) VS(1) VS(2) VS(3) VS(4) VS(5) VS(6) VS(7) VS(8) VS(9) VS(10) VS(11) VS(12) VS(13) VS(14) VS(15)
#undef VS
}

__global__ void __launch_bounds__(768, 3)
fused_layer(const unsigned short* __restrict__ x, const unsigned short* __restrict__ kc, const unsigned short* __restrict__ vc,
            const unsigned short* __restrict__ wq, const unsigned short* __restrict__ wo, const unsigned short* __restrict__ wg,
            const unsigned short* __restrict__ wu, const unsigned short* __restrict__ wd, unsigned short* __restrict__ out, char* ws) {
  __shared__ __attribute__((aligned(16))) char lds[L_END];
  const int bh = blockIdx.x, b = bh >> 5, h = bh & 31;
  const int tid = threadIdx.x, lane = tid & 63, wave = __builtin_amdgcn_readfirstlane(tid >> 6);
  STAMP(0);
  if (wave < 2) {
    __builtin_amdgcn_s_setprio(2);
    const int r = lane >> 4, c = lane & 15;
    const rsrc_t krs = mkrsrc(kc + (size_t)bh * SK * DH, SK * DH * 2), vrs = mkrsrc(vc + (size_t)bh * SK * DH, SK * DH * 2);
    const int voff = tid * 16;
    u32x4 buf[16], ext[16];
#pragma unroll
    for (int i = 0; i < 16; ++i) {
      if (i == N1) BARRIER();
      buf[i] = __builtin_amdgcn_raw_buffer_load_b128(krs, voff, i * 2048, KAUX); SB();
    }
#pragma unroll
    for (int i = 0; i < 16; ++i) { ext[i] = __builtin_amdgcn_raw_buffer_load_b128(krs, voff, (16 + i) * 2048, KAUX); SB(); }
    STAMP(4);
    BARRIER();
    u32x4 q;
    {
      f32x2 s = {0.f, 0.f};
#pragma unroll
      for (int i = 0; i < 10; ++i) s += *(const f32x2*)(lds + L_PART + i * 512 + (8 * c + 2 * r) * 4);
      const unsigned v = pkbf(s[0], s[1]);
      auto a = __builtin_amdgcn_permlane16_swap(v, v, false, false);
      auto e = __builtin_amdgcn_permlane32_swap(a[0], a[0], false, false);
      auto o = __builtin_amdgcn_permlane32_swap(a[1], a[1], false, false);
      q = u32x4{e[0], o[0], e[1], o[1]};
    }
    STAMP(1);
    float* sc = (float*)(lds + L_SC) + wave * 2048 + lane;
    float m, s;
    m = s = kbatch<K_FIRST>(buf, ext, q, krs, vrs, voff, 0, c); sc[0] = s;
    s = kbatch<K_EXT>(buf, ext, q, krs, vrs, voff, 0, c); sc[64] = s; m = fmaxf(m, s);
    s = kbatch<K_RAMP>(buf, ext, q, krs, vrs, voff, 2 * 32768, c); sc[128] = s; m = fmaxf(m, s);
#pragma unroll 1
    for (int bt = 3; bt < 31; ++bt) { s = kbatch<K_LOOP>(buf, ext, q, krs, vrs, voff, bt * 32768, c); sc[bt * 64] = s; m = fmaxf(m, s); }
    s = kbatch<K_LAST>(buf, ext, q, krs, vrs, voff, 31 * 32768, c); sc[31 * 64] = s; m = fmaxf(m, s);
    s = sc[0];
    m = wave_max(m);
    STAMP(2);
    float acc[8], l = 0.f;
#pragma unroll
    for (int j = 0; j < 8; ++j) acc[j] = 0.f;
#pragma unroll 1
    for (int bt = 0; bt < 31; ++bt) {
      const float e = __expf(s - m);
      s = sc[bt * 64 + 64];
      l += e;
      vbatch<false>(buf, acc, f2bf(e), vrs, voff, bt * 32768);
    }
    {
      const float e = __expf(s - m);
      l += e;
      l = wave_sum(l);
      vbatch<true>(buf, acc, f2bf(e), vrs, voff, 31 * 32768);
    }
    *(f32x2*)(lds + L_OP + wave * 512 + (8 * c + 2 * r) * 4) = fold8to2(acc);
    if (lane == 0) *(f32x2*)(lds + L_ML + wave * 8) = f32x2{m, l};
    STAMP(3);
    BARRIER();
    DUMP();
    return;
  }
  const int w = wave - 2, t = tid - 128;
  const int rg = lane >> 3, cc = lane & 7;
  const bool ffn = h < NF;
  float xres;
  {
    const int r = lane >> 4, c = lane & 15;
    const unsigned* x32 = (const unsigned*)(x + b * D);
    unsigned xa[5];
#pragma unroll
    for (int i = 0; i < 5; ++i) xa[i] = x32[lane + 64 * i];
    const int rot = ROT ? 2 * b : 0;
    const float xo = asf((unsigned)x[b * D + 64 * w + 16 * r + ((c + rot) & 15)] << 16);
    xres = asf((unsigned)x[b * D + 20 * h + 4 * (w < 5 ? w : 0) + (lane & 3)] << 16);
    SB();
    const rsrc_t qrs = mkrsrc(wq, D * H * DH * 2);
    u32x4 wv[16];
#pragma unroll
    for (int i = 0; i < 16; ++i) { wv[i] = (WSKIP & 8) ? u32x4{xa[0], xa[1], xa[2], xa[3]} : __builtin_amdgcn_raw_buffer_load_b128(qrs, (64 * w + 16 * r) * 8192 + h * 256 + c * 16, ((i + rot) & 15) * 8192, QAUX); SB(); }
    BARRIER();
    if (t == 0) *(unsigned*)(lds + L_CNT) = 0;
    float s = 0.f;
#pragma unroll
    for (int i = 0; i < 5; ++i) s += asf(xa[i] << 16) + asf(xa[i] & 0xffff0000u);
    const float mean = wave_sum(s) * (1.f / D);
    STAMP(9);
    float ss = 0.f;
#pragma unroll
    for (int i = 0; i < 5; ++i) { const float d0 = asf(xa[i] << 16) - mean, d1 = asf(xa[i] & 0xffff0000u) - mean; ss += d0 * d0 + d1 * d1; }
    const float var = wave_sum(ss) * (1.f / D);
    const float sd = rbf(sqrtf(rbf(rbf(var) + 1e-5f)));
    const unsigned xl = f2bf(rbf(xo - rbf(mean)) / sd), xh = xl << 16;
    STAMP(10);
    float acc[8];
#pragma unroll
    for (int j = 0; j < 8; ++j) acc[j] = 0.f;
#define QS(i) { const unsigned lo = dppu<BC + i>(xl), hi = dppu<BC + i>(xh); \
    _Pragma("unroll") for (int j = 0; j < 4; ++j) { acc[2 * j] = dot2(wv[i][j], lo, acc[2 * j]); acc[2 * j + 1] = dot2(wv[i][j], hi, acc[2 * j + 1]); } }
    QS(0) QS(1) QS(2) QS(3) QS(4) QS(5) QS(6) QS(7) QS(8) QS(9) QS(10) QS(11) QS(12) QS(13) QS(14) QS(15)
#undef QS
    *(f32x2*)(lds + L_PART + w * 512 + (8 * c + 2 * r) * 4) = fold8to2(acc);
  }
  STAMP(11);
  BARRIER();
  STAMP(1);
  {
    const rsrc_t wsr = mkrsrc(ws, WS_BYTES);
    const u32x4 z = {0u, 0u, 0u, 0u};
    __builtin_amdgcn_raw_buffer_store_b128(z, wsr, WS_R + bh * 10240 + t * 16, 0, 16);
    if (t < 10) __builtin_amdgcn_raw_buffer_store_b128(z, wsr, WS_GH + b * 5120 + h * 160 + t * 16, 0, 16);
  }
  const u64 token = ld_agent(ws + WS_EPOCH + b * 256) * 6364136223846793005ull + 1442695040888963407ull;
  unsigned po[8][8], pu[4][8], pd[4][8];
  if (ffn) {
    u32x4 wv[8];
    const int voff = (64 * w + 8 * rg) * (F * 2) + h * 128 + cc * 16;
    {
      const rsrc_t rs = mkrsrc(wg, D * F * 2);
#pragma unroll
      for (int i = 0; i < 8; ++i) { __builtin_amdgcn_s_sleep(PACE); wv[i] = (WSKIP & 1) ? u32x4{0u, 0u, 0u, 0u} : __builtin_amdgcn_raw_buffer_load_b128(rs, voff, i * (F * 2), WAUX); SB(); }
      unsigned pg[4][8];
      interleave<4>(wv, pg);
#pragma unroll
      for (int k = 0; k < 4; ++k) {
        *(u32x4*)(lds + L_GATE + (w * 8 + 2 * k) * 1024 + lane * 16) = u32x4{pg[k][0], pg[k][1], pg[k][2], pg[k][3]};
        *(u32x4*)(lds + L_GATE + (w * 8 + 2 * k + 1) * 1024 + lane * 16) = u32x4{pg[k][4], pg[k][5], pg[k][6], pg[k][7]};
      }
      SB();
    }
    {
      const rsrc_t rs = mkrsrc(wu, D * F * 2);
#pragma unroll
      for (int i = 0; i < 8; ++i) { __builtin_amdgcn_s_sleep(PACE); wv[i] = (WSKIP & 1) ? u32x4{0u, 0u, 0u, 0u} : __builtin_amdgcn_raw_buffer_load_b128(rs, voff, i * (F * 2), WAUX); SB(); }
      interleave<4>(wv, pu);
#pragma unroll
      for (int k = 0; k < 4; ++k)
#pragma unroll
        for (int j = 0; j < 8; ++j) PIN(pu[k][j]);
      SB();
    }
    {
      const rsrc_t rs = mkrsrc(wd, D * F * 2);
#pragma unroll
      for (int i = 0; i < 8; ++i) { __builtin_amdgcn_s_sleep(PACE); wv[i] = (WSKIP & 2) ? u32x4{0u, 0u, 0u, 0u} : __builtin_amdgcn_raw_buffer_load_b128(rs, (64 * h + 8 * rg) * (D * 2) + w * 128 + cc * 16, i * (D * 2), WAUX); SB(); }
      interleave<4>(wv, pd);
#pragma unroll
      for (int k = 0; k < 4; ++k)
#pragma unroll
        for (int j = 0; j < 8; ++j) PIN(pd[k][j]);
      SB();
    }
  } else {
#pragma unroll
    for (int k = 0; k < 4; ++k)
#pragma unroll
      for (int j = 0; j < 8; ++j) pu[k][j] = pd[k][j] = 0u;
  }
  {
    const rsrc_t rs = mkrsrc(wo, H * DH * D * 2);
#pragma unroll
    for (int hf = 0; hf < 2; ++hf) {
      u32x4 wv[8];
#pragma unroll
      for (int i = 0; i < 8; ++i) { __builtin_amdgcn_s_sleep(PACE); wv[i] = (WSKIP & 4) ? u32x4{0u, 0u, 0u, 0u} : __builtin_amdgcn_raw_buffer_load_b128(rs, (128 * h + 16 * rg) * (D * 2) + w * 128 + cc * 16, (8 * hf + i) * (D * 2), WAUX); SB(); }
      unsigned pt[4][8];
      interleave<4>(wv, pt);
#pragma unroll
      for (int k = 0; k < 4; ++k)
#pragma unroll
        for (int j = 0; j < 8; ++j) { po[4 * hf + k][j] = pt[k][j]; PIN(po[4 * hf + k][j]); }
      SB();
    }
  }
  asm volatile("s_waitcnt vmcnt(0)" ::: "memory");
  if (lane == 0) {
    const unsigned old = __hip_atomic_fetch_add((unsigned*)(lds + L_CNT), 1u, __ATOMIC_RELAXED, __HIP_MEMORY_SCOPE_WORKGROUP);
    if (old == 9) st_agent(ws + WS_READY + bh * 8, token);
  }
  if (w == 0) {
  #pragma unroll 1
  for (int spin = 0; spin < SPIN_CAP; ++spin) {
      const u64 v = ld_agent(ws + WS_READY + (b * 32 + (lane & 31)) * 8);
      if (__all(v == token)) break;
      __builtin_amdgcn_s_sleep(16);
    }
    if (h == 0 && lane == 0) st_agent(ws + WS_EPOCH + b * 256, token);
  }
  const int col = 64 * w + 8 * cc + rg, jc = col / 20, rem = col - 20 * jc;
  char* const pub = ws + WS_R + (b * 32 + jc) * 10240 + ((rem >> 2) * 128 + ((h & 7) * 4 + (h >> 3)) * 4 + (rem & 3)) * 8;
  const char* const own = ws + WS_R + bh * 10240 + (w * 128 + lane) * 8;
  STAMP(2);
  BARRIER();
  STAMP(3);
  {
    const f32x2 o0 = *(const f32x2*)(lds + L_OP + lane * 8), o1 = *(const f32x2*)(lds + L_OP + 512 + lane * 8);
    const f32x4 ml = *(const f32x4*)(lds + L_ML);
    const float M = fmaxf(ml[0], ml[2]), f0 = __expf(ml[0] - M), f1 = __expf(ml[2] - M);
    const float inv = 1.f / (ml[1] * f0 + ml[3] * f1), s0 = f0 * inv, s1 = f1 * inv;
    const unsigned pk = pkbf(o0[0] * s0 + o1[0] * s1, o0[1] * s0 + o1[1] * s1);
    const unsigned ok[8] = {bcast8<0>(pk), bcast8<1>(pk), bcast8<2>(pk), bcast8<3>(pk), bcast8<4>(pk), bcast8<5>(pk), bcast8<6>(pk), bcast8<7>(pk)};
    float acc[8];
#pragma unroll
    for (int j = 0; j < 8; ++j) acc[j] = 0.f;
#pragma unroll
    for (int k = 0; k < 8; ++k)
#pragma unroll
      for (int j = 0; j < 8; ++j) acc[j] = dot2(po[k][j], ok[k], acc[j]);
    st_gran(pub, fold8to1(acc, lane));
  }
  STAMP(4);
  u32x4 pg[8];
  if (ffn) {
#pragma unroll
    for (int k = 0; k < 8; ++k) pg[k] = *(const u32x4*)(lds + L_GATE + (w * 8 + k) * 1024 + lane * 16);
  } else if (w >= 5) { DUMP(); return; }
  float hres = 0.f;
  if (w < 5) {
    hres = rbf(xres + rbf(gather32(own, true, true)));
    if (lane < 4) st_gran(ws + WS_GH + (b * 640 + 20 * h + 4 * w + lane) * 8, hres);
  }
  STAMP(5);
  if (ffn) {
    BARRIER();
    u64 g;
  #pragma unroll 1
  for (int spin = 0; spin < SPIN_CAP; ++spin) {
      g = ld_agent(ws + WS_GH + (b * 640 + t) * 8);
      if (__all((unsigned)(g >> 32) == TAG)) break;
    }
    STAMP(6);
    const float hv = asf((unsigned)g);
    const float mw = wave_sum(hv) * (1.f / 64), dv = hv - mw, m2 = wave_sum(dv * dv);
    if (lane == 0) *(f32x2*)(lds + L_ST + w * 8) = f32x2{mw, m2};
    BARRIER();
    f32x4 st[5];
#pragma unroll
    for (int i = 0; i < 5; ++i) st[i] = *(const f32x4*)(lds + L_ST + i * 16);
    float mean = 0.f, s2 = 0.f, sb = 0.f;
#pragma unroll
    for (int i = 0; i < 5; ++i) { mean += st[i][0] + st[i][2]; s2 += st[i][1] + st[i][3]; }
    mean *= 0.1f;
#pragma unroll
    for (int i = 0; i < 5; ++i) { const float d0 = st[i][0] - mean, d1 = st[i][2] - mean; sb += d0 * d0 + d1 * d1; }
    const float var = (s2 + 64.f * sb) * (1.f / D);
    const float sd = rbf(sqrtf(rbf(rbf(var) + 1e-5f)));
    const unsigned hl = f2bf(rbf(hv - rbf(mean)) / sd);
    const unsigned pk = hl | (dppu<XOR1>(hl) << 16);
    const unsigned hk[4] = {bcast8<0>(pk), bcast8<2>(pk), bcast8<4>(pk), bcast8<6>(pk)};
    float ag[8], au[8];
#pragma unroll
    for (int j = 0; j < 8; ++j) ag[j] = au[j] = 0.f;
#pragma unroll
    for (int k = 0; k < 4; ++k)
#pragma unroll
      for (int j = 0; j < 8; ++j) { ag[j] = dot2(pg[2 * k + (j >> 2)][j & 3], hk[k], ag[j]); au[j] = dot2(pu[k][j], hk[k], au[j]); }
    *(f32x2*)(lds + L_PART + w * 512 + (8 * cc + rg) * 8) = f32x2{fold8to1(ag, lane), fold8to1(au, lane)};
    BARRIER();
    if (w == 2) {
      f32x2 s = {0.f, 0.f};
#pragma unroll
      for (int i = 0; i < 10; ++i) s += *(const f32x2*)(lds + L_PART + i * 512 + lane * 8);
      const float gt = rbf(s[0]), up = rbf(s[1]);
      *(unsigned short*)(lds + L_ACT + lane * 2) = (unsigned short)f2bf(rbf(gt / (1.f + __expf(-gt))) * up);
    }
    BARRIER();
    const u32x4 ak = *(const u32x4*)(lds + L_ACT + rg * 16);
    float acc[8];
#pragma unroll
    for (int j = 0; j < 8; ++j) acc[j] = 0.f;
#pragma unroll
    for (int k = 0; k < 4; ++k)
#pragma unroll
      for (int j = 0; j < 8; ++j) acc[j] = dot2(pd[k][j], ak[k], acc[j]);
    st_gran(pub + 5120, fold8to1(acc, lane));
    STAMP(7);
    if (w >= 5) { DUMP(); return; }
  }
  {
    const int s0 = lane >> 2, s1 = s0 + 16;
    const float s = gather32(own + 5120, (s0 >> 2) + 8 * (s0 & 3) < NF, (s1 >> 2) + 8 * (s1 & 3) < NF);
    if (lane < 4) out[b * D + 20 * h + 4 * w + lane] = (unsigned short)f2bf(hres + rbf(s));
  }
  STAMP(8);
  DUMP();
}
}

extern "C" void kernel_launch(void* const* d_in, const int* in_sizes, int n_in, void* d_out, int out_size, void* d_ws, size_t ws_size,
                              hipStream_t stream) {
  fused_layer<<<256, 768, 0, stream>>>((const unsigned short*)d_in[0], (const unsigned short*)d_in[1], (const unsigned short*)d_in[2],
                                       (const unsigned short*)d_in[3], (const unsigned short*)d_in[4], (const unsigned short*)d_in[6],
                                       (const unsigned short*)d_in[7], (const unsigned short*)d_in[8], (unsigned short*)d_out, (char*)d_ws);
}
```

```cpp
#define PROBE 1
#define PBUF vc
#define PSH 13
#include <hip/hip_runtime.h>
#include <cstdint>

#define DIST 11
#define PACE 40
#define WSKIP 0
#define N1 0
#define ROT 1
#define PSLEEP 2
#define QAUX 2
#define KAUX 2
#define VAUX 2
#define WAUX 2

namespace {
constexpr int B = 8, D = 640, H = 32, SK = 4096, DH = 128, F = 1728, NF = 27;
using u32x4 = __attribute__((ext_vector_type(4))) unsigned;
using u32x2 = __attribute__((ext_vector_type(2))) unsigned;
using f32x4 = __attribute__((ext_vector_type(4))) float;
using f32x2 = __attribute__((ext_vector_type(2))) float;
using bf16x2 = __attribute__((ext_vector_type(2))) __bf16;
using u64 = unsigned long long;
typedef __attribute__((address_space(1))) u64 gu64;
typedef __amdgpu_buffer_rsrc_t rsrc_t;

constexpr int WS_EPOCH = 0;
constexpr int WS_READY = 4096;
constexpr int WS_GH = 8192;
constexpr int WS_R = 65536;
constexpr int WS_BYTES = WS_R + 256 * 10240;
constexpr int WS_DIAG = 4 << 20;
constexpr int L_GATE = 0, L_SC = 81920, L_PART = 98304, L_OP = 103424, L_ML = 104448, L_ST = 104464, L_ACT = 104544, L_CNT = 104672, L_END = 104688;
constexpr unsigned TAG = 1u;
constexpr int SPIN_CAP = 1 << 16;

__device__ __forceinline__ float dot2(unsigned a, unsigned b, float acc) {
  return __builtin_amdgcn_fdot2_f32_bf16(__builtin_bit_cast(bf16x2, a), __builtin_bit_cast(bf16x2, b), acc, false);
}
__device__ __forceinline__ float asf(unsigned u) { return __builtin_bit_cast(float, u); }
__device__ __forceinline__ unsigned asu(float f) { return __builtin_bit_cast(unsigned, f); }
__device__ __forceinline__ unsigned f2bf(float f) { return __builtin_bit_cast(unsigned short, (__bf16)f); }
__device__ __forceinline__ float rbf(float f) { return asf(f2bf(f) << 16); }
__device__ __forceinline__ unsigned pkbf(float lo, float hi) { return __builtin_bit_cast(unsigned, bf16x2{(__bf16)lo, (__bf16)hi}); }
template <int CTRL> __device__ __forceinline__ unsigned dppu(unsigned x) { return (unsigned)__builtin_amdgcn_mov_dpp((int)x, CTRL, 0xf, 0xf, true); }
template <int CTRL> __device__ __forceinline__ float dpp(float x) { return asf(dppu<CTRL>(asu(x))); }
constexpr int XOR1 = 0xB1, XOR2 = 0x4E, XOR7 = 0x141, ROR8 = 0x128, ROR4 = 0x124, BC = 0x150;
__device__ __forceinline__ float fold32(float a, float b) {
  auto s = __builtin_amdgcn_permlane32_swap(asu(a), asu(b), false, false);
  return asf(s[0]) + asf(s[1]);
}
__device__ __forceinline__ float fold16(float a, float b) {
  auto s = __builtin_amdgcn_permlane16_swap(asu(a), asu(b), false, false);
  return asf(s[0]) + asf(s[1]);
}
__device__ __forceinline__ float wave_sum(float x) {
  x += dpp<XOR1>(x); x += dpp<XOR2>(x); x += dpp<XOR7>(x); x += dpp<ROR8>(x);
  x = fold16(x, x);
  return fold32(x, x);
}
__device__ __forceinline__ float wave_max(float x) {
  x = fmaxf(x, dpp<XOR1>(x)); x = fmaxf(x, dpp<XOR2>(x)); x = fmaxf(x, dpp<XOR7>(x)); x = fmaxf(x, dpp<ROR8>(x));
  auto s = __builtin_amdgcn_permlane16_swap(asu(x), asu(x), false, false);
  x = fmaxf(asf(s[0]), asf(s[1]));
  auto t = __builtin_amdgcn_permlane32_swap(asu(x), asu(x), false, false);
  return fmaxf(asf(t[0]), asf(t[1]));
}
__device__ __forceinline__ f32x2 fold8to2(const float (&a)[8]) {
  const float t0 = fold32(a[0], a[4]), t1 = fold32(a[1], a[5]), t2 = fold32(a[2], a[6]), t3 = fold32(a[3], a[7]);
  return f32x2{fold16(t0, t2), fold16(t1, t3)};
}
__device__ __forceinline__ float fold8to1(const float (&a)[8], int lane) {
  const f32x2 u = fold8to2(a);
  const float s0 = u[0] + dpp<ROR8>(u[0]), s1 = u[1] + dpp<ROR8>(u[1]);
  return (lane & 8) ? s1 : s0;
}
template <int K> __device__ __forceinline__ unsigned bcast8(unsigned x) { return (unsigned)__builtin_amdgcn_ds_swizzle((int)x, (K << 5) | 0x18); }
__device__ __forceinline__ rsrc_t mkrsrc(const void* p, unsigned bytes) { return __builtin_amdgcn_make_buffer_rsrc((void*)p, 0, bytes, 0x00020000); }
__device__ __forceinline__ u64 ld_agent(const char* p) { return __hip_atomic_load((gu64*)p, __ATOMIC_RELAXED, __HIP_MEMORY_SCOPE_AGENT); }
__device__ __forceinline__ void st_agent(char* p, u64 v) { __hip_atomic_store((gu64*)p, v, __ATOMIC_RELAXED, __HIP_MEMORY_SCOPE_AGENT); }
__device__ __forceinline__ void st_gran(char* p, float v) { st_agent(p, ((u64)TAG << 32) | asu(v)); }
#define SB() __builtin_amdgcn_sched_barrier(0)
#define BARRIER() asm volatile("s_waitcnt lgkmcnt(0)\n\ts_barrier" ::: "memory")
#define PIN(x) asm volatile("" : "+v"(x))
#define GATE(u)
#define STAMP(i) do { SB(); stamp_[i] = __builtin_amdgcn_s_memrealtime(); SB(); } while (0)
#define PWAIT() do { if (wave >= 2 && stamp_[8]) { const u64 e_ = stamp_[0] + 10000 + 1000 * (((u64)(PBUF) >> PSH) & 31); for (int i_ = 0; i_ < 100000; ++i_) { if (__builtin_amdgcn_s_memrealtime() >= e_) break; __builtin_amdgcn_s_sleep(2); } } } while (0)
#define DUMP() PWAIT()

template <int NP> __device__ __forceinline__ void interleave(const u32x4 (&w)[2 * NP], unsigned (&p)[NP][8]) {
#pragma unroll
  for (int k = 0; k < NP; ++k)
#pragma unroll
    for (int j = 0; j < 4; ++j) {
      p[k][2 * j] = __builtin_amdgcn_perm(w[2 * k + 1][j], w[2 * k][j], 0x05040100u);
      p[k][2 * j + 1] = __builtin_amdgcn_perm(w[2 * k + 1][j], w[2 * k][j], 0x07060302u);
    }
}

__device__ __forceinline__ float gather32(const char* g, bool need0, bool need1) {
  u64 v0 = (u64)TAG << 32, v1 = (u64)TAG << 32;
#pragma unroll 1
  for (int spin = 0; spin < SPIN_CAP; ++spin) {
    if (need0) v0 = ld_agent(g);
    if (need1) v1 = ld_agent(g + 512);
    need0 = (unsigned)(v0 >> 32) != TAG; need1 = (unsigned)(v1 >> 32) != TAG;
    if (!__any(need0 || need1)) break;
    __builtin_amdgcn_s_sleep(PSLEEP);
  }
  float s = asf((unsigned)v0) + asf((unsigned)v1);
  s += dpp<ROR8>(s); s += dpp<ROR4>(s);
  s = fold16(s, s);
  return fold32(s, s);
}

enum { K_FIRST, K_EXT, K_RAMP, K_LOOP, K_LAST };
template <int MODE>
__device__ __forceinline__ float kbatch(u32x4 (&buf)[16], const u32x4 (&ext)[16], const u32x4 q, rsrc_t krs, rsrc_t vrs, int voff, int soff, int c) {
  float xx[16];
#pragma unroll
  for (int i = 0; i < 16; ++i) {
    const u32x4 k = MODE == K_EXT ? ext[i] : buf[i];
    float d0 = dot2(k[0], q[0], 0.f), d1 = dot2(k[1], q[1], 0.f);
    d0 = dot2(k[2], q[2], d0); d1 = dot2(k[3], q[3], d1);
    xx[i] = d0 + d1;
    PIN(xx[i]);
    SB();
    if (MODE == K_FIRST) buf[i] = __builtin_amdgcn_raw_buffer_load_b128(krs, voff, (32 + i) * 2048, KAUX);
    if (MODE == K_LOOP || (MODE == K_RAMP && i + DIST >= 16) || (MODE == K_LAST && i + DIST < 16))
      buf[(i + DIST) & 15] = __builtin_amdgcn_raw_buffer_load_b128(krs, voff, soff + (i + DIST) * 2048, KAUX);
    if (MODE == K_LAST && i + DIST >= 16) buf[(i + DIST) & 15] = __builtin_amdgcn_raw_buffer_load_b128(vrs, voff, (i + DIST - 16) * 2048, VAUX);
    SB();
  }
#define TR_STEP(HALF, CTRL, BIT) _Pragma("unroll") for (int hh = 0; hh < (HALF); ++hh) { \
    const float lo_ = xx[hh], hi_ = xx[hh + (HALF)]; const float t1_ = lo_ + dpp<CTRL>(lo_), t2_ = hi_ + dpp<CTRL>(hi_); \
    xx[hh] = (c & (BIT)) ? t2_ : t1_; }
  TR_STEP(8, ROR8, 8) TR_STEP(4, XOR7, 4) TR_STEP(2, XOR2, 2) TR_STEP(1, XOR1, 1)
#undef TR_STEP
  return rbf(rbf(xx[0]) * 0.08838834764831845f);
}
template <int I, bool LAST>
__device__ __forceinline__ void vstep(u32x4 (&buf)[16], float (&acc)[8], unsigned pl, rsrc_t vrs, int voff, int soff) {
  const u32x4 v = buf[I];
  const unsigned lo = dppu<BC + I>(pl), hi = lo << 16;
#pragma unroll
  for (int j = 0; j < 4; ++j) { acc[2 * j] = dot2(v[j], lo, acc[2 * j]); acc[2 * j + 1] = dot2(v[j], hi, acc[2 * j + 1]); }
#pragma unroll
  for (int j = 0; j < 8; ++j) PIN(acc[j]);
  SB();
  if (!LAST || I + DIST < 16) buf[(I + DIST) & 15] = __builtin_amdgcn_raw_buffer_load_b128(vrs, voff, soff + (I + DIST) * 2048, VAUX);
  SB();
}
template <bool LAST>
__device__ __forceinline__ void vbatch(u32x4 (&buf)[16], float (&acc)[8], unsigned pl, rsrc_t vrs, int voff, int soff) {
#define VS(i) vstep<i, LAST>(buf, acc, pl, vrs, voff, soff);
  VS(0) VS(1) VS(2) VS(3) VS(4) VS(5) VS(6) VS(7) VS(8) VS(9) VS(10) VS(11) VS(12) VS(13) VS(14) VS(15)
#undef VS
}

__global__ void __launch_bounds__(768, 3)
fused_layer(const unsigned short* __restrict__ x, const unsigned short* __restrict__ kc, const unsigned short* __restrict__ vc,
            const unsigned short* __restrict__ wq, const unsigned short* __restrict__ wo, const unsigned short* __restrict__ wg,
            const unsigned short* __restrict__ wu, const unsigned short* __restrict__ wd, unsigned short* __restrict__ out, char* ws) {
  __shared__ __attribute__((aligned(16))) char lds[L_END];
  const int bh = blockIdx.x, b = bh >> 5, h = bh & 31;
  const int tid = threadIdx.x, lane = tid & 63, wave = __builtin_amdgcn_readfirstlane(tid >> 6);
  u64 stamp_[12] = {0, 0, 0, 0, 0, 0, 0, 0, 0, 0, 0, 0};
  STAMP(0);
  if (wave < 2) {
    __builtin_amdgcn_s_setprio(2);
    const int r = lane >> 4, c = lane & 15;
    const rsrc_t krs = mkrsrc(kc + (size_t)bh * SK * DH, SK * DH * 2), vrs = mkrsrc(vc + (size_t)bh * SK * DH, SK * DH * 2);
    const int voff = tid * 16;
    u32x4 buf[16], ext[16];
#pragma unroll
    for (int i = 0; i < 16; ++i) {
      if (i == N1) BARRIER();
      buf[i] = __builtin_amdgcn_raw_buffer_load_b128(krs, voff, i * 2048, KAUX); SB();
    }
#pragma unroll
    for (int i = 0; i < 16; ++i) { ext[i] = __builtin_amdgcn_raw_buffer_load_b128(krs, voff, (16 + i) * 2048, KAUX); SB(); }
    STAMP(4);
    BARRIER();
    u32x4 q;
    {
      f32x2 s = {0.f, 0.f};
#pragma unroll
      for (int i = 0; i < 10; ++i) s += *(const f32x2*)(lds + L_PART + i * 512 + (8 * c + 2 * r) * 4);
      const unsigned v = pkbf(s[0], s[1]);
      auto a = __builtin_amdgcn_permlane16_swap(v, v, false, false);
      auto e = __builtin_amdgcn_permlane32_swap(a[0], a[0], false, false);
      auto o = __builtin_amdgcn_permlane32_swap(a[1], a[1], false, false);
      q = u32x4{e[0], o[0], e[1], o[1]};
    }
    STAMP(1);
    float* sc = (float*)(lds + L_SC) + wave * 2048 + lane;
    float m, s;
    m = s = kbatch<K_FIRST>(buf, ext, q, krs, vrs, voff, 0, c); sc[0] = s;
    s = kbatch<K_EXT>(buf, ext, q, krs, vrs, voff, 0, c); sc[64] = s; m = fmaxf(m, s);
    s = kbatch<K_RAMP>(buf, ext, q, krs, vrs, voff, 2 * 32768, c); sc[128] = s; m = fmaxf(m, s);
#pragma unroll 1
    for (int bt = 3; bt < 31; ++bt) { s = kbatch<K_LOOP>(buf, ext, q, krs, vrs, voff, bt * 32768, c); sc[bt * 64] = s; m = fmaxf(m, s); GATE(bt + 1); }
    s = kbatch<K_LAST>(buf, ext, q, krs, vrs, voff, 31 * 32768, c); sc[31 * 64] = s; m = fmaxf(m, s);
    s = sc[0];
    m = wave_max(m);
    STAMP(2);
    float acc[8], l = 0.f;
#pragma unroll
    for (int j = 0; j < 8; ++j) acc[j] = 0.f;
#pragma unroll 1
    for (int bt = 0; bt < 31; ++bt) {
      const float e = __expf(s - m);
      s = sc[bt * 64 + 64];
      l += e;
      vbatch<false>(buf, acc, f2bf(e), vrs, voff, bt * 32768);
      GATE(bt + 33);
    }
    {
      const float e = __expf(s - m);
      l += e;
      l = wave_sum(l);
      vbatch<true>(buf, acc, f2bf(e), vrs, voff, 31 * 32768);
    }
    *(f32x2*)(lds + L_OP + wave * 512 + (8 * c + 2 * r) * 4) = fold8to2(acc);
    if (lane == 0) *(f32x2*)(lds + L_ML + wave * 8) = f32x2{m, l};
    STAMP(3);
    BARRIER();
    DUMP();
    return;
  }
  const int w = wave - 2, t = tid - 128;
  const int rg = lane >> 3, cc = lane & 7;
  const bool ffn = h < NF;
  float xres;
  {
    const int r = lane >> 4, c = lane & 15;
    const unsigned* x32 = (const unsigned*)(x + b * D);
    unsigned xa[5];
#pragma unroll
    for (int i = 0; i < 5; ++i) xa[i] = x32[lane + 64 * i];
    const int rot = ROT ? 2 * b : 0;
    const float xo = asf((unsigned)x[b * D + 64 * w + 16 * r + ((c + rot) & 15)] << 16);
    xres = asf((unsigned)x[b * D + 20 * h + 4 * (w < 5 ? w : 0) + (lane & 3)] << 16);
    SB();
    const rsrc_t qrs = mkrsrc(wq, D * H * DH * 2);
    u32x4 wv[16];
#pragma unroll
    for (int i = 0; i < 16; ++i) { wv[i] = (WSKIP & 8) ? u32x4{xa[0], xa[1], xa[2], xa[3]} : __builtin_amdgcn_raw_buffer_load_b128(qrs, (64 * w + 16 * r) * 8192 + h * 256 + c * 16, ((i + rot) & 15) * 8192, QAUX); SB(); }
    BARRIER();
    if (t == 0) *(unsigned*)(lds + L_CNT) = 0;
    float s = 0.f;
#pragma unroll
    for (int i = 0; i < 5; ++i) s += asf(xa[i] << 16) + asf(xa[i] & 0xffff0000u);
    const float mean = wave_sum(s) * (1.f / D);
    STAMP(9);
    float ss = 0.f;
#pragma unroll
    for (int i = 0; i < 5; ++i) { const float d0 = asf(xa[i] << 16) - mean, d1 = asf(xa[i] & 0xffff0000u) - mean; ss += d0 * d0 + d1 * d1; }
    const float var = wave_sum(ss) * (1.f / D);
    const float sd = rbf(sqrtf(rbf(rbf(var) + 1e-5f)));
    const unsigned xl = f2bf(rbf(xo - rbf(mean)) / sd), xh = xl << 16;
    STAMP(10);
    float acc[8];
#pragma unroll
    for (int j = 0; j < 8; ++j) acc[j] = 0.f;
#define QS(i) { const unsigned lo = dppu<BC + i>(xl), hi = dppu<BC + i>(xh); \
    _Pragma("unroll") for (int j = 0; j < 4; ++j) { acc[2 * j] = dot2(wv[i][j], lo, acc[2 * j]); acc[2 * j + 1] = dot2(wv[i][j], hi, acc[2 * j + 1]); } }
    QS(0) QS(1) QS(2) QS(3) QS(4) QS(5) QS(6) QS(7) QS(8) QS(9) QS(10) QS(11) QS(12) QS(13) QS(14) QS(15)
#undef QS
    *(f32x2*)(lds + L_PART + w * 512 + (8 * c + 2 * r) * 4) = fold8to2(acc);
  }
  STAMP(11);
  BARRIER();
  STAMP(1);
  {
    const rsrc_t wsr = mkrsrc(ws, WS_BYTES);
    const u32x4 z = {0u, 0u, 0u, 0u};
    __builtin_amdgcn_raw_buffer_store_b128(z, wsr, WS_R + bh * 10240 + t * 16, 0, 16);
    if (t < 10) __builtin_amdgcn_raw_buffer_store_b128(z, wsr, WS_GH + b * 5120 + h * 160 + t * 16, 0, 16);
  }
  const u64 token = ld_agent(ws + WS_EPOCH + b * 256) * 6364136223846793005ull + 1442695040888963407ull;
  unsigned po[8][8], pu[4][8], pd[4][8];
  if (ffn) {
    u32x4 wv[8];
    const int voff = (64 * w + 8 * rg) * (F * 2) + h * 128 + cc * 16;
    {
      const rsrc_t rs = mkrsrc(wg, D * F * 2);
#pragma unroll
      for (int i = 0; i < 8; ++i) { __builtin_amdgcn_s_sleep(PACE); wv[i] = (WSKIP & 1) ? u32x4{0u, 0u, 0u, 0u} : __builtin_amdgcn_raw_buffer_load_b128(rs, voff, i * (F * 2), WAUX); SB(); }
      unsigned pg[4][8];
      interleave<4>(wv, pg);
#pragma unroll
      for (int k = 0; k < 4; ++k) {
        *(u32x4*)(lds + L_GATE + (w * 8 + 2 * k) * 1024 + lane * 16) = u32x4{pg[k][0], pg[k][1], pg[k][2], pg[k][3]};
        *(u32x4*)(lds + L_GATE + (w * 8 + 2 * k + 1) * 1024 + lane * 16) = u32x4{pg[k][4], pg[k][5], pg[k][6], pg[k][7]};
      }
      SB();
    }
    {
      const rsrc_t rs = mkrsrc(wu, D * F * 2);
#pragma unroll
      for (int i = 0; i < 8; ++i) { __builtin_amdgcn_s_sleep(PACE); wv[i] = (WSKIP & 1) ? u32x4{0u, 0u, 0u, 0u} : __builtin_amdgcn_raw_buffer_load_b128(rs, voff, i * (F * 2), WAUX); SB(); }
      interleave<4>(wv, pu);
#pragma unroll
      for (int k = 0; k < 4; ++k)
#pragma unroll
        for (int j = 0; j < 8; ++j) PIN(pu[k][j]);
      SB();
    }
    {
      const rsrc_t rs = mkrsrc(wd, D * F * 2);
#pragma unroll
      for (int i = 0; i < 8; ++i) { __builtin_amdgcn_s_sleep(PACE); wv[i] = (WSKIP & 2) ? u32x4{0u, 0u, 0u, 0u} : __builtin_amdgcn_raw_buffer_load_b128(rs, (64 * h + 8 * rg) * (D * 2) + w * 128 + cc * 16, i * (D * 2), WAUX); SB(); }
      interleave<4>(wv, pd);
#pragma unroll
      for (int k = 0; k < 4; ++k)
#pragma unroll
        for (int j = 0; j < 8; ++j) PIN(pd[k][j]);
      SB();
    }
  } else {
#pragma unroll
    for (int k = 0; k < 4; ++k)
#pragma unroll
      for (int j = 0; j < 8; ++j) pu[k][j] = pd[k][j] = 0u;
  }
  {
    const rsrc_t rs = mkrsrc(wo, H * DH * D * 2);
#pragma unroll
    for (int hf = 0; hf < 2; ++hf) {
      u32x4 wv[8];
#pragma unroll
      for (int i = 0; i < 8; ++i) { __builtin_amdgcn_s_sleep(PACE); wv[i] = (WSKIP & 4) ? u32x4{0u, 0u, 0u, 0u} : __builtin_amdgcn_raw_buffer_load_b128(rs, (128 * h + 16 * rg) * (D * 2) + w * 128 + cc * 16, (8 * hf + i) * (D * 2), WAUX); SB(); }
      unsigned pt[4][8];
      interleave<4>(wv, pt);
#pragma unroll
      for (int k = 0; k < 4; ++k)
#pragma unroll
        for (int j = 0; j < 8; ++j) { po[4 * hf + k][j] = pt[k][j]; PIN(po[4 * hf + k][j]); }
      SB();
    }
  }
  asm volatile("s_waitcnt vmcnt(0)" ::: "memory");
  if (lane == 0) {
    const unsigned old = __hip_atomic_fetch_add((unsigned*)(lds + L_CNT), 1u, __ATOMIC_RELAXED, __HIP_MEMORY_SCOPE_WORKGROUP);
    if (old == 9) st_agent(ws + WS_READY + bh * 8, token);
  }
  if (w == 0) {
  #pragma unroll 1
  for (int spin = 0; spin < SPIN_CAP; ++spin) {
      const u64 v = ld_agent(ws + WS_READY + (b * 32 + (lane & 31)) * 8);
      if (__all(v == token)) break;
      __builtin_amdgcn_s_sleep(16);
    }
    if (h == 0 && lane == 0) st_agent(ws + WS_EPOCH + b * 256, token);
  }
  const int col = 64 * w + 8 * cc + rg, jc = col / 20, rem = col - 20 * jc;
  char* const pub = ws + WS_R + (b * 32 + jc) * 10240 + ((rem >> 2) * 128 + ((h & 7) * 4 + (h >> 3)) * 4 + (rem & 3)) * 8;
  const char* const own = ws + WS_R + bh * 10240 + (w * 128 + lane) * 8;
  STAMP(2);
  BARRIER();
  STAMP(3);
  {
    const f32x2 o0 = *(const f32x2*)(lds + L_OP + lane * 8), o1 = *(const f32x2*)(lds + L_OP + 512 + lane * 8);
    const f32x4 ml = *(const f32x4*)(lds + L_ML);
    const float M = fmaxf(ml[0], ml[2]), f0 = __expf(ml[0] - M), f1 = __expf(ml[2] - M);
    const float inv = 1.f / (ml[1] * f0 + ml[3] * f1), s0 = f0 * inv, s1 = f1 * inv;
    const unsigned pk = pkbf(o0[0] * s0 + o1[0] * s1, o0[1] * s0 + o1[1] * s1);
    const unsigned ok[8] = {bcast8<0>(pk), bcast8<1>(pk), bcast8<2>(pk), bcast8<3>(pk), bcast8<4>(pk), bcast8<5>(pk), bcast8<6>(pk), bcast8<7>(pk)};
    float acc[8];
#pragma unroll
    for (int j = 0; j < 8; ++j) acc[j] = 0.f;
#pragma unroll
    for (int k = 0; k < 8; ++k)
#pragma unroll
      for (int j = 0; j < 8; ++j) acc[j] = dot2(po[k][j], ok[k], acc[j]);
    st_gran(pub, fold8to1(acc, lane));
  }
  STAMP(4);
  u32x4 pg[8];
  if (ffn) {
#pragma unroll
    for (int k = 0; k < 8; ++k) pg[k] = *(const u32x4*)(lds + L_GATE + (w * 8 + k) * 1024 + lane * 16);
  } else if (w >= 5) { DUMP(); return; }
  float hres = 0.f;
  if (w < 5) {
    hres = rbf(xres + rbf(gather32(own, true, true)));
    if (lane < 4) st_gran(ws + WS_GH + (b * 640 + 20 * h + 4 * w + lane) * 8, hres);
  }
  STAMP(5);
  if (ffn) {
    BARRIER();
    u64 g;
  #pragma unroll 1
  for (int spin = 0; spin < SPIN_CAP; ++spin) {
      g = ld_agent(ws + WS_GH + (b * 640 + t) * 8);
      if (__all((unsigned)(g >> 32) == TAG)) break;
    }
    STAMP(6);
    const float hv = asf((unsigned)g);
    const float mw = wave_sum(hv) * (1.f / 64), dv = hv - mw, m2 = wave_sum(dv * dv);
    if (lane == 0) *(f32x2*)(lds + L_ST + w * 8) = f32x2{mw, m2};
    BARRIER();
    f32x4 st[5];
#pragma unroll
    for (int i = 0; i < 5; ++i) st[i] = *(const f32x4*)(lds + L_ST + i * 16);
    float mean = 0.f, s2 = 0.f, sb = 0.f;
#pragma unroll
    for (int i = 0; i < 5; ++i) { mean += st[i][0] + st[i][2]; s2 += st[i][1] + st[i][3]; }
    mean *= 0.1f;
#pragma unroll
    for (int i = 0; i < 5; ++i) { const float d0 = st[i][0] - mean, d1 = st[i][2] - mean; sb += d0 * d0 + d1 * d1; }
    const float var = (s2 + 64.f * sb) * (1.f / D);
    const float sd = rbf(sqrtf(rbf(rbf(var) + 1e-5f)));
    const unsigned hl = f2bf(rbf(hv - rbf(mean)) / sd);
    const unsigned pk = hl | (dppu<XOR1>(hl) << 16);
    const unsigned hk[4] = {bcast8<0>(pk), bcast8<2>(pk), bcast8<4>(pk), bcast8<6>(pk)};
    float ag[8], au[8];
#pragma unroll
    for (int j = 0; j < 8; ++j) ag[j] = au[j] = 0.f;
#pragma unroll
    for (int k = 0; k < 4; ++k)
#pragma unroll
      for (int j = 0; j < 8; ++j) { ag[j] = dot2(pg[2 * k + (j >> 2)][j & 3], hk[k], ag[j]); au[j] = dot2(pu[k][j], hk[k], au[j]); }
    *(f32x2*)(lds + L_PART + w * 512 + (8 * cc + rg) * 8) = f32x2{fold8to1(ag, lane), fold8to1(au, lane)};
    BARRIER();
    if (w == 2) {
      f32x2 s = {0.f, 0.f};
#pragma unroll
      for (int i = 0; i < 10; ++i) s += *(const f32x2*)(lds + L_PART + i * 512 + lane * 8);
      const float gt = rbf(s[0]), up = rbf(s[1]);
      *(unsigned short*)(lds + L_ACT + lane * 2) = (unsigned short)f2bf(rbf(gt / (1.f + __expf(-gt))) * up);
    }
    BARRIER();
    const u32x4 ak = *(const u32x4*)(lds + L_ACT + rg * 16);
    float acc[8];
#pragma unroll
    for (int j = 0; j < 8; ++j) acc[j] = 0.f;
#pragma unroll
    for (int k = 0; k < 4; ++k)
#pragma unroll
      for (int j = 0; j < 8; ++j) acc[j] = dot2(pd[k][j], ak[k], acc[j]);
    st_gran(pub + 5120, fold8to1(acc, lane));
    STAMP(7);
    if (w >= 5) { DUMP(); return; }
  }
  {
    const int s0 = lane >> 2, s1 = s0 + 16;
    const float s = gather32(own + 5120, (s0 >> 2) + 8 * (s0 & 3) < NF, (s1 >> 2) + 8 * (s1 & 3) < NF);
    if (lane < 4) out[b * D + 20 * h + 4 * w + lane] = (unsigned short)f2bf(hres + rbf(s));
  }
  STAMP(8);
  DUMP();
}
}

extern "C" void kernel_launch(void* const* d_in, const int* in_sizes, int n_in, void* d_out, int out_size, void* d_ws, size_t ws_size,
                              hipStream_t stream) {
  fused_layer<<<256, 768, 0, stream>>>((const unsigned short*)d_in[0], (const unsigned short*)d_in[1], (const unsigned short*)d_in[2],
                                       (const unsigned short*)d_in[3], (const unsigned short*)d_in[4], (const unsigned short*)d_in[6],
                                       (const unsigned short*)d_in[7], (const unsigned short*)d_in[8], (unsigned short*)d_out, (char*)d_ws);
}
```

```cpp
#define OSCOPE __HIP_MEMORY_SCOPE_AGENT
#include <hip/hip_runtime.h>
#include <cstdint>

#define DIST 11
#define PACE 40
#define WSKIP 0
#define N1 0
#define ROT 1
#define PSLEEP 2
#define QAUX 2
#define KAUX 2
#define VAUX 2
#define SSCOPE __HIP_MEMORY_SCOPE_AGENT
#define CAUX 16
#define WAUX 2

namespace {
constexpr int B = 8, D = 640, H = 32, SK = 4096, DH = 128, F = 1728, NF = 27;
using u32x4 = __attribute__((ext_vector_type(4))) unsigned;
using u32x2 = __attribute__((ext_vector_type(2))) unsigned;
using f32x4 = __attribute__((ext_vector_type(4))) float;
using f32x2 = __attribute__((ext_vector_type(2))) float;
using bf16x2 = __attribute__((ext_vector_type(2))) __bf16;
using u64 = unsigned long long;
typedef __attribute__((address_space(1))) u64 gu64;
typedef __amdgpu_buffer_rsrc_t rsrc_t;

constexpr int WS_EPOCH = 0;
constexpr int WS_READY = 4096;
constexpr int WS_GH = 8192;
constexpr int WS_R = 65536;
constexpr int WS_BYTES = WS_R + 256 * 10240;
constexpr int WS_DIAG = 4 << 20;
constexpr int L_GATE = 0, L_SC = 81920, L_PART = 98304, L_OP = 103424, L_ML = 104448, L_ST = 104464, L_ACT = 104544, L_CNT = 104672, L_END = 104688;
constexpr unsigned TAG = 1u;
constexpr int SPIN_CAP = 1 << 16;

__device__ __forceinline__ float dot2(unsigned a, unsigned b, float acc) {
  return __builtin_amdgcn_fdot2_f32_bf16(__builtin_bit_cast(bf16x2, a), __builtin_bit_cast(bf16x2, b), acc, false);
}
__device__ __forceinline__ float asf(unsigned u) { return __builtin_bit_cast(float, u); }
__device__ __forceinline__ unsigned asu(float f) { return __builtin_bit_cast(unsigned, f); }
__device__ __forceinline__ unsigned f2bf(float f) { return __builtin_bit_cast(unsigned short, (__bf16)f); }
__device__ __forceinline__ float rbf(float f) { return asf(f2bf(f) << 16); }
__device__ __forceinline__ unsigned pkbf(float lo, float hi) { return __builtin_bit_cast(unsigned, bf16x2{(__bf16)lo, (__bf16)hi}); }
template <int CTRL> __device__ __forceinline__ unsigned dppu(unsigned x) { return (unsigned)__builtin_amdgcn_mov_dpp((int)x, CTRL, 0xf, 0xf, true); }
template <int CTRL> __device__ __forceinline__ float dpp(float x) { return asf(dppu<CTRL>(asu(x))); }
constexpr int XOR1 = 0xB1, XOR2 = 0x4E, XOR7 = 0x141, ROR8 = 0x128, ROR4 = 0x124, BC = 0x150;
__device__ __forceinline__ float fold32(float a, float b) {
  auto s = __builtin_amdgcn_permlane32_swap(asu(a), asu(b), false, false);
  return asf(s[0]) + asf(s[1]);
}
__device__ __forceinline__ float fold16(float a, float b) {
  auto s = __builtin_amdgcn_permlane16_swap(asu(a), asu(b), false, false);
  return asf(s[0]) + asf(s[1]);
}
__device__ __forceinline__ float wave_sum(float x) {
  x += dpp<XOR1>(x); x += dpp<XOR2>(x); x += dpp<XOR7>(x); x += dpp<ROR8>(x);
  x = fold16(x, x);
  return fold32(x, x);
}
__device__ __forceinline__ float wave_max(float x) {
  x = fmaxf(x, dpp<XOR1>(x)); x = fmaxf(x, dpp<XOR2>(x)); x = fmaxf(x, dpp<XOR7>(x)); x = fmaxf(x, dpp<ROR8>(x));
  auto s = __builtin_amdgcn_permlane16_swap(asu(x), asu(x), false, false);
  x = fmaxf(asf(s[0]), asf(s[1]));
  auto t = __builtin_amdgcn_permlane32_swap(asu(x), asu(x), false, false);
  return fmaxf(asf(t[0]), asf(t[1]));
}
__device__ __forceinline__ f32x2 fold8to2(const float (&a)[8]) {
  const float t0 = fold32(a[0], a[4]), t1 = fold32(a[1], a[5]), t2 = fold32(a[2], a[6]), t3 = fold32(a[3], a[7]);
  return f32x2{fold16(t0, t2), fold16(t1, t3)};
}
__device__ __forceinline__ float fold8to1(const float (&a)[8], int lane) {
  const f32x2 u = fold8to2(a);
  const float s0 = u[0] + dpp<ROR8>(u[0]), s1 = u[1] + dpp<ROR8>(u[1]);
  return (lane & 8) ? s1 : s0;
}
template <int K> __device__ __forceinline__ unsigned bcast8(unsigned x) { return (unsigned)__builtin_amdgcn_ds_swizzle((int)x, (K << 5) | 0x18); }
__device__ __forceinline__ rsrc_t mkrsrc(const void* p, unsigned bytes) { return __builtin_amdgcn_make_buffer_rsrc((void*)p, 0, bytes, 0x00020000); }
__device__ __forceinline__ u64 ld_agent(const char* p) { return __hip_atomic_load((gu64*)p, __ATOMIC_RELAXED, __HIP_MEMORY_SCOPE_AGENT); }
__device__ __forceinline__ void st_agent(char* p, u64 v) { __hip_atomic_store((gu64*)p, v, __ATOMIC_RELAXED, SSCOPE); }
__device__ __forceinline__ void st_gran(char* p, float v) { st_agent(p, ((u64)TAG << 32) | asu(v)); }
#define SB() __builtin_amdgcn_sched_barrier(0)
#define BARRIER() asm volatile("s_waitcnt lgkmcnt(0)\n\ts_barrier" ::: "memory")
#define PIN(x) asm volatile("" : "+v"(x))
#define GATE(u)
#define STAMP(i)
#define PWAIT()
#define DUMP() PWAIT()

template <int NP> __device__ __forceinline__ void interleave(const u32x4 (&w)[2 * NP], unsigned (&p)[NP][8]) {
#pragma unroll
  for (int k = 0; k < NP; ++k)
#pragma unroll
    for (int j = 0; j < 4; ++j) {
      p[k][2 * j] = __builtin_amdgcn_perm(w[2 * k + 1][j], w[2 * k][j], 0x05040100u);
      p[k][2 * j + 1] = __builtin_amdgcn_perm(w[2 * k + 1][j], w[2 * k][j], 0x07060302u);
    }
}

__device__ __forceinline__ float gather32(const char* g, bool need0, bool need1) {
  u64 v0 = (u64)TAG << 32, v1 = (u64)TAG << 32;
#pragma unroll 1
  for (int spin = 0; spin < SPIN_CAP; ++spin) {
    if (need0) v0 = ld_agent(g);
    if (need1) v1 = ld_agent(g + 512);
    need0 = (unsigned)(v0 >> 32) != TAG; need1 = (unsigned)(v1 >> 32) != TAG;
    if (!__any(need0 || need1)) break;
    __builtin_amdgcn_s_sleep(PSLEEP);
  }
  float s = asf((unsigned)v0) + asf((unsigned)v1);
  s += dpp<ROR8>(s); s += dpp<ROR4>(s);
  s = fold16(s, s);
  return fold32(s, s);
}

enum { K_FIRST, K_EXT, K_RAMP, K_LOOP, K_LAST };
template <int MODE>
__device__ __forceinline__ float kbatch(u32x4 (&buf)[16], const u32x4 (&ext)[16], const u32x4 q, rsrc_t krs, rsrc_t vrs, int voff, int soff, int c) {
  float xx[16];
#pragma unroll
  for (int i = 0; i < 16; ++i) {
    const u32x4 k = MODE == K_EXT ? ext[i] : buf[i];
    float d0 = dot2(k[0], q[0], 0.f), d1 = dot2(k[1], q[1], 0.f);
    d0 = dot2(k[2], q[2], d0); d1 = dot2(k[3], q[3], d1);
    xx[i] = d0 + d1;
    PIN(xx[i]);
    SB();
    if (MODE == K_FIRST) buf[i] = __builtin_amdgcn_raw_buffer_load_b128(krs, voff, (32 + i) * 2048, KAUX);
    if (MODE == K_LOOP || (MODE == K_RAMP && i + DIST >= 16) || (MODE == K_LAST && i + DIST < 16))
      buf[(i + DIST) & 15] = __builtin_amdgcn_raw_buffer_load_b128(krs, voff, soff + (i + DIST) * 2048, KAUX);
    if (MODE == K_LAST && i + DIST >= 16) buf[(i + DIST) & 15] = __builtin_amdgcn_raw_buffer_load_b128(vrs, voff, (i + DIST - 16) * 2048, VAUX);
    SB();
  }
#define TR_STEP(HALF, CTRL, BIT) _Pragma("unroll") for (int hh = 0; hh < (HALF); ++hh) { \
    const float lo_ = xx[hh], hi_ = xx[hh + (HALF)]; const float t1_ = lo_ + dpp<CTRL>(lo_), t2_ = hi_ + dpp<CTRL>(hi_); \
    xx[hh] = (c & (BIT)) ? t2_ : t1_; }
  TR_STEP(8, ROR8, 8) TR_STEP(4, XOR7, 4) TR_STEP(2, XOR2, 2) TR_STEP(1, XOR1, 1)
#undef TR_STEP
  return rbf(rbf(xx[0]) * 0.08838834764831845f);
}
template <int I, bool LAST>
__device__ __forceinline__ void vstep(u32x4 (&buf)[16], float (&acc)[8], unsigned pl, rsrc_t vrs, int voff, int soff) {
  const u32x4 v = buf[I];
  const unsigned lo = dppu<BC + I>(pl), hi = lo << 16;
#pragma unroll
  for (int j = 0; j < 4; ++j) { acc[2 * j] = dot2(v[j], lo, acc[2 * j]); acc[2 * j + 1] = dot2(v[j], hi, acc[2 * j + 1]); }
#pragma unroll
  for (int j = 0; j < 8; ++j) PIN(acc[j]);
  SB();
  if (!LAST || I + DIST < 16) buf[(I + DIST) & 15] = __builtin_amdgcn_raw_buffer_load_b128(vrs, voff, soff + (I + DIST) * 2048, VAUX);
  SB();
}
template <bool LAST>
__device__ __forceinline__ void vbatch(u32x4 (&buf)[16], float (&acc)[8], unsigned pl, rsrc_t vrs, int voff, int soff) {
#define VS(i) vstep<i, LAST>(buf, acc, pl, vrs, voff, soff);
  VS(0) VS(1) VS(2) VS(3) VS(4) VS(5) VS(6) VS(7) VS(8) VS(9) VS(10) VS(11) VS(12) VS(13) VS(14) VS(15)
#undef VS
}

__global__ void __launch_bounds__(768, 3)
fused_layer(const unsigned short* __restrict__ x, const unsigned short* __restrict__ kc, const unsigned short* __restrict__ vc,
            const unsigned short* __restrict__ wq, const unsigned short* __restrict__ wo, const unsigned short* __restrict__ wg,
            const unsigned short* __restrict__ wu, const unsigned short* __restrict__ wd, unsigned short* __restrict__ out, char* ws) {
  __shared__ __attribute__((aligned(16))) char lds[L_END];
  const int bh = blockIdx.x, b = bh >> 5, h = bh & 31;
  const int tid = threadIdx.x, lane = tid & 63, wave = __builtin_amdgcn_readfirstlane(tid >> 6);
  STAMP(0);
  if (wave < 2) {
    __builtin_amdgcn_s_setprio(2);
    const int r = lane >> 4, c = lane & 15;
    const rsrc_t krs = mkrsrc(kc + (size_t)bh * SK * DH, SK * DH * 2), vrs = mkrsrc(vc + (size_t)bh * SK * DH, SK * DH * 2);
    const int voff = tid * 16;
    u32x4 buf[16], ext[16];
#pragma unroll
    for (int i = 0; i < 16; ++i) {
      if (i == N1) BARRIER();
      buf[i] = __builtin_amdgcn_raw_buffer_load_b128(krs, voff, i * 2048, KAUX); SB();
    }
#pragma unroll
    for (int i = 0; i < 16; ++i) { ext[i] = __builtin_amdgcn_raw_buffer_load_b128(krs, voff, (16 + i) * 2048, KAUX); SB(); }
    STAMP(4);
    BARRIER();
    u32x4 q;
    {
      f32x2 s = {0.f, 0.f};
#pragma unroll
      for (int i = 0; i < 10; ++i) s += *(const f32x2*)(lds + L_PART + i * 512 + (8 * c + 2 * r) * 4);
      const unsigned v = pkbf(s[0], s[1]);
      auto a = __builtin_amdgcn_permlane16_swap(v, v, false, false);
      auto e = __builtin_amdgcn_permlane32_swap(a[0], a[0], false, false);
      auto o = __builtin_amdgcn_permlane32_swap(a[1], a[1], false, false);
      q = u32x4{e[0], o[0], e[1], o[1]};
    }
    STAMP(1);
    float* sc = (float*)(lds + L_SC) + wave * 2048 + lane;
    float m, s;
    m = s = kbatch<K_FIRST>(buf, ext, q, krs, vrs, voff, 0, c); sc[0] = s;
    s = kbatch<K_EXT>(buf, ext, q, krs, vrs, voff, 0, c); sc[64] = s; m = fmaxf(m, s);
    s = kbatch<K_RAMP>(buf, ext, q, krs, vrs, voff, 2 * 32768, c); sc[128] = s; m = fmaxf(m, s);
#pragma unroll 1
    for (int bt = 3; bt < 31; ++bt) { s = kbatch<K_LOOP>(buf, ext, q, krs, vrs, voff, bt * 32768, c); sc[bt * 64] = s; m = fmaxf(m, s); GATE(bt + 1); }
    s = kbatch<K_LAST>(buf, ext, q, krs, vrs, voff, 31 * 32768, c); sc[31 * 64] = s; m = fmaxf(m, s);
    s = sc[0];
    m = wave_max(m);
    STAMP(2);
    float acc[8], l = 0.f;
#pragma unroll
    for (int j = 0; j < 8; ++j) acc[j] = 0.f;
#pragma unroll 1
    for (int bt = 0; bt < 31; ++bt) {
      const float e = __expf(s - m);
      s = sc[bt * 64 + 64];
      l += e;
      vbatch<false>(buf, acc, f2bf(e), vrs, voff, bt * 32768);
      GATE(bt + 33);
    }
    {
      const float e = __expf(s - m);
      l += e;
      l = wave_sum(l);
      vbatch<true>(buf, acc, f2bf(e), vrs, voff, 31 * 32768);
    }
    *(f32x2*)(lds + L_OP + wave * 512 + (8 * c + 2 * r) * 4) = fold8to2(acc);
    if (lane == 0) *(f32x2*)(lds + L_ML + wave * 8) = f32x2{m, l};
    STAMP(3);
    BARRIER();
    DUMP();
    return;
  }
  const int w = wave - 2, t = tid - 128;
  const int rg = lane >> 3, cc = lane & 7;
  const bool ffn = h < NF;
  float xres;
  {
    const int r = lane >> 4, c = lane & 15;
    const unsigned* x32 = (const unsigned*)(x + b * D);
    unsigned xa[5];
#pragma unroll
    for (int i = 0; i < 5; ++i) xa[i] = x32[lane + 64 * i];
    const int rot = ROT ? 2 * b : 0;
    const float xo = asf((unsigned)x[b * D + 64 * w + 16 * r + ((c + rot) & 15)] << 16);
    xres = asf((unsigned)x[b * D + 20 * h + 4 * (w < 5 ? w : 0) + (lane & 3)] << 16);
    SB();
    const rsrc_t qrs = mkrsrc(wq, D * H * DH * 2);
    u32x4 wv[16];
#pragma unroll
    for (int i = 0; i < 16; ++i) { wv[i] = (WSKIP & 8) ? u32x4{xa[0], xa[1], xa[2], xa[3]} : __builtin_amdgcn_raw_buffer_load_b128(qrs, (64 * w + 16 * r) * 8192 + h * 256 + c * 16, ((i + rot) & 15) * 8192, QAUX); SB(); }
    BARRIER();
    if (t == 0) *(unsigned*)(lds + L_CNT) = 0;
    float s = 0.f;
#pragma unroll
    for (int i = 0; i < 5; ++i) s += asf(xa[i] << 16) + asf(xa[i] & 0xffff0000u);
    const float mean = wave_sum(s) * (1.f / D);
    STAMP(9);
    float ss = 0.f;
#pragma unroll
    for (int i = 0; i < 5; ++i) { const float d0 = asf(xa[i] << 16) - mean, d1 = asf(xa[i] & 0xffff0000u) - mean; ss += d0 * d0 + d1 * d1; }
    const float var = wave_sum(ss) * (1.f / D);
    const float sd = rbf(sqrtf(rbf(rbf(var) + 1e-5f)));
    const unsigned xl = f2bf(rbf(xo - rbf(mean)) / sd), xh = xl << 16;
    STAMP(10);
    float acc[8];
#pragma unroll
    for (int j = 0; j < 8; ++j) acc[j] = 0.f;
#define QS(i) { const unsigned lo = dppu<BC + i>(xl), hi = dppu<BC + i>(xh); \
    _Pragma("unroll") for (int j = 0; j < 4; ++j) { acc[2 * j] = dot2(wv[i][j], lo, acc[2 * j]); acc[2 * j + 1] = dot2(wv[i][j], hi, acc[2 * j + 1]); } }
    QS(0) QS(1) QS(2) QS(3) QS(4) QS(5) QS(6) QS(7) QS(8) QS(9) QS(10) QS(11) QS(12) QS(13) QS(14) QS(15)
#undef QS
    *(f32x2*)(lds + L_PART + w * 512 + (8 * c + 2 * r) * 4) = fold8to2(acc);
  }
  STAMP(11);
  BARRIER();
  STAMP(1);
  {
    const rsrc_t wsr = mkrsrc(ws, WS_BYTES);
    const u32x4 z = {0u, 0u, 0u, 0u};
    __builtin_amdgcn_raw_buffer_store_b128(z, wsr, WS_R + bh * 10240 + t * 16, 0, CAUX);
    if (t < 10) __builtin_amdgcn_raw_buffer_store_b128(z, wsr, WS_GH + b * 5120 + h * 160 + t * 16, 0, CAUX);
  }
  const u64 token = ld_agent(ws + WS_EPOCH + b * 256) * 6364136223846793005ull + 1442695040888963407ull;
  unsigned po[8][8], pu[4][8], pd[4][8];
  if (ffn) {
    u32x4 wv[8];
    const int voff = (64 * w + 8 * rg) * (F * 2) + h * 128 + cc * 16;
    {
      const rsrc_t rs = mkrsrc(wg, D * F * 2);
#pragma unroll
      for (int i = 0; i < 8; ++i) { __builtin_amdgcn_s_sleep(PACE); wv[i] = (WSKIP & 1) ? u32x4{0u, 0u, 0u, 0u} : __builtin_amdgcn_raw_buffer_load_b128(rs, voff, i * (F * 2), WAUX); SB(); }
      unsigned pg[4][8];
      interleave<4>(wv, pg);
#pragma unroll
      for (int k = 0; k < 4; ++k) {
        *(u32x4*)(lds + L_GATE + (w * 8 + 2 * k) * 1024 + lane * 16) = u32x4{pg[k][0], pg[k][1], pg[k][2], pg[k][3]};
        *(u32x4*)(lds + L_GATE + (w * 8 + 2 * k + 1) * 1024 + lane * 16) = u32x4{pg[k][4], pg[k][5], pg[k][6], pg[k][7]};
      }
      SB();
    }
    {
      const rsrc_t rs = mkrsrc(wu, D * F * 2);
#pragma unroll
      for (int i = 0; i < 8; ++i) { __builtin_amdgcn_s_sleep(PACE); wv[i] = (WSKIP & 1) ? u32x4{0u, 0u, 0u, 0u} : __builtin_amdgcn_raw_buffer_load_b128(rs, voff, i * (F * 2), WAUX); SB(); }
      interleave<4>(wv, pu);
#pragma unroll
      for (int k = 0; k < 4; ++k)
#pragma unroll
        for (int j = 0; j < 8; ++j) PIN(pu[k][j]);
      SB();
    }
    {
      const rsrc_t rs = mkrsrc(wd, D * F * 2);
#pragma unroll
      for (int i = 0; i < 8; ++i) { __builtin_amdgcn_s_sleep(PACE); wv[i] = (WSKIP & 2) ? u32x4{0u, 0u, 0u, 0u} : __builtin_amdgcn_raw_buffer_load_b128(rs, (64 * h + 8 * rg) * (D * 2) + w * 128 + cc * 16, i * (D * 2), WAUX); SB(); }
      interleave<4>(wv, pd);
#pragma unroll
      for (int k = 0; k < 4; ++k)
#pragma unroll
        for (int j = 0; j < 8; ++j) PIN(pd[k][j]);
      SB();
    }
  } else {
#pragma unroll
    for (int k = 0; k < 4; ++k)
#pragma unroll
      for (int j = 0; j < 8; ++j) pu[k][j] = pd[k][j] = 0u;
  }
  {
    const rsrc_t rs = mkrsrc(wo, H * DH * D * 2);
#pragma unroll
    for (int hf = 0; hf < 2; ++hf) {
      u32x4 wv[8];
#pragma unroll
      for (int i = 0; i < 8; ++i) { __builtin_amdgcn_s_sleep(PACE); wv[i] = (WSKIP & 4) ? u32x4{0u, 0u, 0u, 0u} : __builtin_amdgcn_raw_buffer_load_b128(rs, (128 * h + 16 * rg) * (D * 2) + w * 128 + cc * 16, (8 * hf + i) * (D * 2), WAUX); SB(); }
      unsigned pt[4][8];
      interleave<4>(wv, pt);
#pragma unroll
      for (int k = 0; k < 4; ++k)
#pragma unroll
        for (int j = 0; j < 8; ++j) { po[4 * hf + k][j] = pt[k][j]; PIN(po[4 * hf + k][j]); }
      SB();
    }
  }
  asm volatile("s_waitcnt vmcnt(0)" ::: "memory");
  if (lane == 0) {
    const unsigned old = __hip_atomic_fetch_add((unsigned*)(lds + L_CNT), 1u, __ATOMIC_RELAXED, __HIP_MEMORY_SCOPE_WORKGROUP);
    if (old == 9) st_agent(ws + WS_READY + bh * 8, token);
  }
  if (w == 0) {
  #pragma unroll 1
  for (int spin = 0; spin < SPIN_CAP; ++spin) {
      const u64 v = ld_agent(ws + WS_READY + (b * 32 + (lane & 31)) * 8);
      if (__all(v == token)) break;
      __builtin_amdgcn_s_sleep(16);
    }
    if (h == 0 && lane == 0) st_agent(ws + WS_EPOCH + b * 256, token);
  }
  const int col = 64 * w + 8 * cc + rg, jc = col / 20, rem = col - 20 * jc;
  char* const pub = ws + WS_R + (b * 32 + jc) * 10240 + ((rem >> 2) * 128 + ((h & 7) * 4 + (h >> 3)) * 4 + (rem & 3)) * 8;
  const char* const own = ws + WS_R + bh * 10240 + (w * 128 + lane) * 8;
  STAMP(2);
  BARRIER();
  STAMP(3);
  {
    const f32x2 o0 = *(const f32x2*)(lds + L_OP + lane * 8), o1 = *(const f32x2*)(lds + L_OP + 512 + lane * 8);
    const f32x4 ml = *(const f32x4*)(lds + L_ML);
    const float M = fmaxf(ml[0], ml[2]), f0 = __expf(ml[0] - M), f1 = __expf(ml[2] - M);
    const float inv = 1.f / (ml[1] * f0 + ml[3] * f1), s0 = f0 * inv, s1 = f1 * inv;
    const unsigned pk = pkbf(o0[0] * s0 + o1[0] * s1, o0[1] * s0 + o1[1] * s1);
    const unsigned ok[8] = {bcast8<0>(pk), bcast8<1>(pk), bcast8<2>(pk), bcast8<3>(pk), bcast8<4>(pk), bcast8<5>(pk), bcast8<6>(pk), bcast8<7>(pk)};
    float acc[8];
#pragma unroll
    for (int j = 0; j < 8; ++j) acc[j] = 0.f;
#pragma unroll
    for (int k = 0; k < 8; ++k)
#pragma unroll
      for (int j = 0; j < 8; ++j) acc[j] = dot2(po[k][j], ok[k], acc[j]);
    st_gran(pub, fold8to1(acc, lane));
  }
  STAMP(4);
  u32x4 pg[8];
  if (ffn) {
#pragma unroll
    for (int k = 0; k < 8; ++k) pg[k] = *(const u32x4*)(lds + L_GATE + (w * 8 + k) * 1024 + lane * 16);
  } else if (w >= 5) { DUMP(); return; }
  float hres = 0.f;
  if (w < 5) {
    hres = rbf(xres + rbf(gather32(own, true, true)));
    if (lane < 4) st_gran(ws + WS_GH + (b * 640 + 20 * h + 4 * w + lane) * 8, hres);
  }
  STAMP(5);
  if (ffn) {
    BARRIER();
    u64 g;
  #pragma unroll 1
  for (int spin = 0; spin < SPIN_CAP; ++spin) {
      g = ld_agent(ws + WS_GH + (b * 640 + t) * 8);
      if (__all((unsigned)(g >> 32) == TAG)) break;
    }
    STAMP(6);
    const float hv = asf((unsigned)g);
    const float mw = wave_sum(hv) * (1.f / 64), dv = hv - mw, m2 = wave_sum(dv * dv);
    if (lane == 0) *(f32x2*)(lds + L_ST + w * 8) = f32x2{mw, m2};
    BARRIER();
    f32x4 st[5];
#pragma unroll
    for (int i = 0; i < 5; ++i) st[i] = *(const f32x4*)(lds + L_ST + i * 16);
    float mean = 0.f, s2 = 0.f, sb = 0.f;
#pragma unroll
    for (int i = 0; i < 5; ++i) { mean += st[i][0] + st[i][2]; s2 += st[i][1] + st[i][3]; }
    mean *= 0.1f;
#pragma unroll
    for (int i = 0; i < 5; ++i) { const float d0 = st[i][0] - mean, d1 = st[i][2] - mean; sb += d0 * d0 + d1 * d1; }
    const float var = (s2 + 64.f * sb) * (1.f / D);
    const float sd = rbf(sqrtf(rbf(rbf(var) + 1e-5f)));
    const unsigned hl = f2bf(rbf(hv - rbf(mean)) / sd);
    const unsigned pk = hl | (dppu<XOR1>(hl) << 16);
    const unsigned hk[4] = {bcast8<0>(pk), bcast8<2>(pk), bcast8<4>(pk), bcast8<6>(pk)};
    float ag[8], au[8];
#pragma unroll
    for (int j = 0; j < 8; ++j) ag[j] = au[j] = 0.f;
#pragma unroll
    for (int k = 0; k < 4; ++k)
#pragma unroll
      for (int j = 0; j < 8; ++j) { ag[j] = dot2(pg[2 * k + (j >> 2)][j & 3], hk[k], ag[j]); au[j] = dot2(pu[k][j], hk[k], au[j]); }
    *(f32x2*)(lds + L_PART + w * 512 + (8 * cc + rg) * 8) = f32x2{fold8to1(ag, lane), fold8to1(au, lane)};
    BARRIER();
    if (w == 2) {
      f32x2 s = {0.f, 0.f};
#pragma unroll
      for (int i = 0; i < 10; ++i) s += *(const f32x2*)(lds + L_PART + i * 512 + lane * 8);
      const float gt = rbf(s[0]), up = rbf(s[1]);
      *(unsigned short*)(lds + L_ACT + lane * 2) = (unsigned short)f2bf(rbf(gt / (1.f + __expf(-gt))) * up);
    }
    BARRIER();
    const u32x4 ak = *(const u32x4*)(lds + L_ACT + rg * 16);
    float acc[8];
#pragma unroll
    for (int j = 0; j < 8; ++j) acc[j] = 0.f;
#pragma unroll
    for (int k = 0; k < 4; ++k)
#pragma unroll
      for (int j = 0; j < 8; ++j) acc[j] = dot2(pd[k][j], ak[k], acc[j]);
    st_gran(pub + 5120, fold8to1(acc, lane));
    STAMP(7);
    if (w >= 5) { DUMP(); return; }
  }
  {
    const int s0 = lane >> 2, s1 = s0 + 16;
    const float s = gather32(own + 5120, (s0 >> 2) + 8 * (s0 & 3) < NF, (s1 >> 2) + 8 * (s1 & 3) < NF);
    if (lane < 4) __hip_atomic_store(out + b * D + 20 * h + 4 * w + lane, (unsigned short)f2bf(hres + rbf(s)), __ATOMIC_RELAXED, OSCOPE);
  }
  STAMP(8);
  DUMP();
}
}

extern "C" void kernel_launch(void* const* d_in, const int* in_sizes, int n_in, void* d_out, int out_size, void* d_ws, size_t ws_size,
                              hipStream_t stream) {
  fused_layer<<<256, 768, 0, stream>>>((const unsigned short*)d_in[0], (const unsigned short*)d_in[1], (const unsigned short*)d_in[2],
                                       (const unsigned short*)d_in[3], (const unsigned short*)d_in[4], (const unsigned short*)d_in[6],
                                       (const unsigned short*)d_in[7], (const unsigned short*)d_in[8], (unsigned short*)d_out, (char*)d_ws);
}
```

```cpp
#include <hip/hip_runtime.h>
#include <cstdint>

constexpr int p_tau = 921, p_toff = 250, p_tau2 = 912;
namespace {
constexpr int B = 8, D = 640, H = 32, SK = 4096, DH = 128, F = 1728, NF = 27;
using u32x8 = __attribute__((ext_vector_type(8))) unsigned;
using u32x4 = __attribute__((ext_vector_type(4))) unsigned;
using u32x2 = __attribute__((ext_vector_type(2))) unsigned;
using f32x4 = __attribute__((ext_vector_type(4))) float;
using f32x2 = __attribute__((ext_vector_type(2))) float;
using bf16x2 = __attribute__((ext_vector_type(2))) __bf16;
using bf16x8 = __attribute__((ext_vector_type(8))) __bf16;
using bf16x16 = __attribute__((ext_vector_type(16))) __bf16;
using u64 = unsigned long long;
typedef __attribute__((address_space(1))) u64 gu64;
typedef __attribute__((address_space(3))) volatile int lds_int;
typedef __amdgpu_buffer_rsrc_t rsrc_t;

constexpr int WS_EPOCH = 0;
constexpr int WS_READY = 65536;
constexpr int WS_GH = 8192;
constexpr int WS_R = 262144;
constexpr int WS_BYTES = WS_R + 256 * 5120;
constexpr int L_GATE = 0, L_SC = 81920, L_PART = 98304, L_CS = 103424, L_OP = 108544, L_ACT = 109056, L_ML = 109312, L_TOK = 109328, L_SH = 109336, L_END = 109344, L_H = L_SC;
constexpr unsigned EMPTY = 0xffffffffu;
constexpr int SPIN_CAP = 1 << 16;
constexpr int DIST = 12;
constexpr int PACE = 36;
constexpr int RSTR = 4352, NREP = 4;
constexpr int DPOS = 6;
constexpr int NT = 2, DEV_LD = 16, DEV_ST = 17;

__device__ __forceinline__ float dot2(unsigned a, unsigned b, float acc) {
  return __builtin_amdgcn_fdot2_f32_bf16(__builtin_bit_cast(bf16x2, a), __builtin_bit_cast(bf16x2, b), acc, false);
}
__device__ __forceinline__ f32x4 smf(u32x4 a, u32x8 b, f32x4 c, int idx) {
  return __builtin_amdgcn_smfmac_f32_16x16x64_bf16(__builtin_bit_cast(bf16x8, a), __builtin_bit_cast(bf16x16, b), c, idx, 0, 0);
}
__device__ __forceinline__ float asf(unsigned u) { return __builtin_bit_cast(float, u); }
__device__ __forceinline__ unsigned asu(float f) { return __builtin_bit_cast(unsigned, f); }
__device__ __forceinline__ unsigned f2bf(float f) { return __builtin_bit_cast(unsigned short, (__bf16)f); }
__device__ __forceinline__ float rbf(float f) { return asf(f2bf(f) << 16); }
__device__ __forceinline__ unsigned pkbf(float lo, float hi) { return __builtin_bit_cast(unsigned, bf16x2{(__bf16)lo, (__bf16)hi}); }
template <int CTRL> __device__ __forceinline__ unsigned dppu(unsigned x) { return (unsigned)__builtin_amdgcn_mov_dpp((int)x, CTRL, 0xf, 0xf, true); }
template <int CTRL> __device__ __forceinline__ float dpp(float x) { return asf(dppu<CTRL>(asu(x))); }
constexpr int XOR1 = 0xB1, XOR2 = 0x4E, XOR7 = 0x141, ROR8 = 0x128, ROR4 = 0x124, BC = 0x150;
__device__ __forceinline__ float fold32(float a, float b) {
  auto s = __builtin_amdgcn_permlane32_swap(asu(a), asu(b), false, false);
  return asf(s[0]) + asf(s[1]);
}
__device__ __forceinline__ float fold16(float a, float b) {
  auto s = __builtin_amdgcn_permlane16_swap(asu(a), asu(b), false, false);
  return asf(s[0]) + asf(s[1]);
}
__device__ __forceinline__ float wave_sum(float x) {
  x += dpp<XOR1>(x); x += dpp<XOR2>(x); x += dpp<XOR7>(x); x += dpp<ROR8>(x);
  x = fold16(x, x);
  return fold32(x, x);
}
__device__ __forceinline__ float wave_max(float x) {
  x = fmaxf(x, dpp<XOR1>(x)); x = fmaxf(x, dpp<XOR2>(x)); x = fmaxf(x, dpp<XOR7>(x)); x = fmaxf(x, dpp<ROR8>(x));
  auto s = __builtin_amdgcn_permlane16_swap(asu(x), asu(x), false, false);
  x = fmaxf(asf(s[0]), asf(s[1]));
  auto t = __builtin_amdgcn_permlane32_swap(asu(x), asu(x), false, false);
  return fmaxf(asf(t[0]), asf(t[1]));
}
__device__ __forceinline__ f32x2 fold8to2(const float (&a)[8]) {
  const float t0 = fold32(a[0], a[4]), t1 = fold32(a[1], a[5]), t2 = fold32(a[2], a[6]), t3 = fold32(a[3], a[7]);
  return f32x2{fold16(t0, t2), fold16(t1, t3)};
}
__device__ __forceinline__ rsrc_t mkrsrc(const void* p, unsigned bytes) { return __builtin_amdgcn_make_buffer_rsrc((void*)p, 0, bytes, 0x00020000); }
__device__ __forceinline__ u64 ld_agent(const char* p) { return __hip_atomic_load((gu64*)p, __ATOMIC_RELAXED, __HIP_MEMORY_SCOPE_AGENT); }
__device__ __forceinline__ void st_agent(char* p, u64 v) { __hip_atomic_store((gu64*)p, v, __ATOMIC_RELAXED, __HIP_MEMORY_SCOPE_AGENT); }
#define SB() __builtin_amdgcn_sched_barrier(0)
#define BARRIER() asm volatile("s_waitcnt lgkmcnt(0)\n\ts_barrier" ::: "memory")
#define PIN(x) asm volatile("" : "+v"(x))
#define FENCE() asm volatile("" ::: "memory")

template <int NS, int ROWB> __device__ __forceinline__ void load_tile(rsrc_t rs, int voff, u32x8 (&w)[NS][2]) {
#pragma unroll
  for (int s = 0; s < NS; ++s) {
    u32x2 v[8];
#pragma unroll
    for (int i = 0; i < 8; ++i) {
      if (!(i & 1)) __builtin_amdgcn_s_sleep(PACE);
      v[i] = __builtin_amdgcn_raw_buffer_load_b64(rs, voff, (32 * s + 16 * (i >> 2) + (i & 3)) * ROWB, NT);
      SB();
    }
#pragma unroll
    for (int j = 0; j < 8; ++j) {
      w[s][0][j] = __builtin_amdgcn_perm(v[(j & 6) + 1][j & 1], v[j & 6][j & 1], 0x05040100u);
      w[s][1][j] = __builtin_amdgcn_perm(v[(j & 6) + 1][j & 1], v[j & 6][j & 1], 0x07060302u);
    }
    PIN(w[s][0]); PIN(w[s][1]);
    SB();
  }
}
__device__ __forceinline__ float tile_col(const u32x8 (&w)[2][2], u32x4 a0, u32x4 a1, int idx, int lane) {
  f32x4 c0 = {0.f, 0.f, 0.f, 0.f}, c1 = c0;
  c0 = smf(a0, w[0][0], c0, idx); c1 = smf(a0, w[0][1], c1, idx);
  c0 = smf(a1, w[1][0], c0, idx); c1 = smf(a1, w[1][1], c1, idx);
  return lane & 32 ? c1[0] : c0[0];
}

__device__ __forceinline__ bool missing(u32x4 v) { return max(max(v[0], v[1]), max(v[2], v[3])) == EMPTY; }
template <int NP> __device__ __forceinline__ float gather(rsrc_t rs, int voff, int soff, int r) {
  constexpr int NL = (NP + 3) / 4;
  u32x4 v[NL];
  const int vlast = 4 * NL - 1 < NP || 4 * NL - 4 + r < NP ? voff : WS_BYTES;
#pragma unroll
  for (int i = 0; i < NL; ++i) { v[i] = __builtin_amdgcn_raw_buffer_load_b128(rs, i == NL - 1 ? vlast : voff, soff + i * 1024, DEV_LD); SB(); }
  FENCE();
  auto incomplete = [&]() {
    bool need = false;
#pragma unroll
    for (int i = 0; i < NL; ++i) need |= missing(v[i]);
    return __any(need);
  };
#pragma unroll
  for (int i = 0; i < NL; ++i) PIN(v[i]);
  if (incomplete()) {
#pragma unroll 1
    for (int spin = 0; spin < SPIN_CAP; ++spin) {
#pragma unroll
      for (int i = 0; i < NL; ++i) if (missing(v[i])) v[i] = __builtin_amdgcn_raw_buffer_load_b128(rs, voff, soff + i * 1024, DEV_LD);
      FENCE();
      if (!incomplete()) break;
    }
  }
  f32x4 s = __builtin_bit_cast(f32x4, v[0]), u = __builtin_bit_cast(f32x4, v[1]);
#pragma unroll
  for (int i = 2; i < NL; ++i) { if (i & 1) u += __builtin_bit_cast(f32x4, v[i]); else s += __builtin_bit_cast(f32x4, v[i]); }
  s += u;
  return fold16(fold32(s[0], s[2]), fold32(s[1], s[3]));
}
__device__ __forceinline__ u32x2 pack_quad(float y) {
  auto s = __builtin_amdgcn_permlane32_swap(asu(y), asu(y), false, false);
  const unsigned pk = __builtin_amdgcn_perm(s[1], s[0], 0x07060302u);
  auto t = __builtin_amdgcn_permlane16_swap(pk, pk, false, false);
  return u32x2{t[0], t[1]};
}

__device__ __forceinline__ u64 now() { return __builtin_amdgcn_s_memrealtime(); }
__device__ __forceinline__ void gate(u64 tm, unsigned tgt) {
  asm volatile("" : "+s"(tm));
  if (__builtin_expect((int)((unsigned)tm - tgt) < 0, 0)) {
    do { __builtin_amdgcn_s_sleep(1); } while ((int)((unsigned)now() - tgt) < 0);
  }
  SB();
}
enum { K_FIRST, K_EXT, K_RAMP, K_LOOP, K_LAST };
template <int MODE>
__device__ __forceinline__ float kbatch(u32x4 (&buf)[16], const u32x4 (&ext)[16], const u32x4 q, rsrc_t krs, rsrc_t vrs, int voff, int soff, int c, unsigned tgt = 0) {
  float xx[16];
  u64 tm = 0;
#pragma unroll
  for (int i = 0; i < 16; ++i) {
    const u32x4 k = MODE == K_EXT ? ext[i] : buf[i];
    float d0 = dot2(k[0], q[0], 0.f), d1 = dot2(k[1], q[1], 0.f);
    d0 = dot2(k[2], q[2], d0); d1 = dot2(k[3], q[3], d1);
    xx[i] = d0 + d1;
    PIN(xx[i]);
    SB();
    if (MODE == K_LOOP && i % 8 == 0) tm = now();
    if (MODE == K_LOOP && i % 8 == 6) gate(tm, tgt + (i - 10) * 7);
    if (MODE == K_FIRST) buf[i] = __builtin_amdgcn_raw_buffer_load_b128(krs, voff, (32 + i) * 2048, NT);
    if (MODE == K_LOOP || (MODE == K_RAMP && i + DIST >= 16) || (MODE == K_LAST && i + DIST < 16))
      buf[(i + DIST) & 15] = __builtin_amdgcn_raw_buffer_load_b128(krs, voff, soff + (i + DIST) * 2048, NT);
    if (MODE == K_LAST && i + DIST >= 16) buf[(i + DIST) & 15] = __builtin_amdgcn_raw_buffer_load_b128(vrs, voff, (i + DIST - 16) * 2048, NT);
    SB();
  }
#define TR_STEP(HALF, CTRL, BIT) _Pragma("unroll") for (int hh = 0; hh < (HALF); ++hh) { \
    const float lo_ = xx[hh], hi_ = xx[hh + (HALF)]; const float t1_ = lo_ + dpp<CTRL>(lo_), t2_ = hi_ + dpp<CTRL>(hi_); \
    xx[hh] = (c & (BIT)) ? t2_ : t1_; }
  TR_STEP(8, ROR8, 8) TR_STEP(4, XOR7, 4) TR_STEP(2, XOR2, 2) TR_STEP(1, XOR1, 1)
#undef TR_STEP
  return rbf(rbf(xx[0]) * 0.08838834764831845f);
}
template <int I, bool LAST>
__device__ __forceinline__ void vstep(u32x4 (&buf)[16], float (&acc)[8], unsigned pl, rsrc_t vrs, int voff, int soff) {
  const u32x4 v = buf[I];
  const unsigned lo = dppu<BC + I>(pl), hi = lo << 16;
#pragma unroll
  for (int j = 0; j < 4; ++j) { acc[2 * j] = dot2(v[j], lo, acc[2 * j]); acc[2 * j + 1] = dot2(v[j], hi, acc[2 * j + 1]); }
#pragma unroll
  for (int j = 0; j < 8; ++j) PIN(acc[j]);
  SB();
  if (!LAST || I + DIST < 16) buf[(I + DIST) & 15] = __builtin_amdgcn_raw_buffer_load_b128(vrs, voff, soff + (I + DIST) * 2048, NT);
  SB();
}
template <bool LAST, int I0, int I1>
__device__ __forceinline__ void vbatch(u32x4 (&buf)[16], float (&acc)[8], unsigned pl, rsrc_t vrs, int voff, int soff, unsigned tgt = 0) {
  u64 tm = 0;
#define VS(i) if (I0 <= i && i < I1) { vstep<i, LAST>(buf, acc, pl, vrs, voff, soff); \
  if (!LAST && i % 8 == 0) { tm = now(); SB(); } if (!LAST && i % 8 == 6) { gate(tm, tgt + (i - 10) * 7); SB(); } }
  VS(0) VS(1) VS(2) VS(3) VS(4) VS(5) VS(6) VS(7) VS(8) VS(9) VS(10) VS(11) VS(12) VS(13) VS(14) VS(15)
#undef VS
}

__global__ void __launch_bounds__(768, 3)
fused_layer(const unsigned short* __restrict__ x, const unsigned short* __restrict__ kc, const unsigned short* __restrict__ vc,
            const unsigned short* __restrict__ wq, const unsigned short* __restrict__ wo, const unsigned short* __restrict__ wg,
            const unsigned short* __restrict__ wu, const unsigned short* __restrict__ wd, unsigned short* __restrict__ out, char* ws) {
  __shared__ __attribute__((aligned(16))) char lds[L_END];
  const int bh = blockIdx.x, b = bh >> 5, id = bh & 31;
  const int tid = threadIdx.x, lane = tid & 63, wave = __builtin_amdgcn_readfirstlane(tid >> 6);
  const int r = lane >> 4, c = lane & 15;
  const bool ffn = id < NF;
  const rsrc_t wsr = mkrsrc(ws, WS_BYTES);
  asm volatile(".Lpf_b:");
  if (wave < 2) {
    __builtin_amdgcn_s_setprio(2);
    const unsigned t0 = (unsigned)now() + p_toff;
    const rsrc_t krs = mkrsrc(kc + (size_t)bh * SK * DH, SK * DH * 2), vrs = mkrsrc(vc + (size_t)bh * SK * DH, SK * DH * 2);
    const int voff = tid * 16;
    if (wave == 0) {
      const u64 token = ld_agent(ws + WS_EPOCH + b * 256) * 6364136223846793005ull + 1442695040888963407ull;
      const u32x4 e = {EMPTY, EMPTY, EMPTY, EMPTY};
#pragma unroll
      for (int i = 0; i < 5; ++i) __builtin_amdgcn_raw_buffer_store_b128(e, wsr, voff, WS_R + bh * 5120 + i * 1024, DEV_ST);
      if (id < 20 && lane < 16) __builtin_amdgcn_raw_buffer_store_b128(e, wsr, voff, WS_GH + b * 5120 + id * 256, DEV_ST);
      asm volatile("s_waitcnt vmcnt(0)" ::: "memory");
      const u64 word = (token & ~0xffffull) | (t0 & 0xffffu);
      if (lane == 0) { st_agent(ws + WS_READY + b * RSTR + id * 8, word); *(u64*)(lds + L_TOK) = word; *(int*)(lds + L_SH) = 0; }
    }
    if (wave == 1 && bh < 16) {
      u64 pc; unsigned back, len;
      asm volatile("s_getpc_b64 %0\n.Lpf_c:\n\ts_mov_b32 %1, .Lpf_c - .Lpf_b\n\ts_mov_b32 %2, .Lpf_z - .Lpf_b" : "=s"(pc), "=s"(back), "=s"(len));
      const rsrc_t crs = mkrsrc((const void*)(pc - back), len < 32768 ? len : 0);
      const unsigned a0 = __builtin_amdgcn_raw_buffer_load_b32(crs, lane * 128, 0, 0), a1 = __builtin_amdgcn_raw_buffer_load_b32(crs, lane * 128, 8192, 0), a2 = __builtin_amdgcn_raw_buffer_load_b32(crs, lane * 128, 16384, 0);
      asm volatile("" ::"v"(a0), "v"(a1), "v"(a2));
    }
    u32x4 buf[16], ext[16];
    BARRIER();
#pragma unroll
    for (int i = 0; i < 16; ++i) { buf[i] = __builtin_amdgcn_raw_buffer_load_b128(krs, voff, i * 2048, NT); SB(); }
#pragma unroll
    for (int i = 0; i < 16; ++i) { ext[i] = __builtin_amdgcn_raw_buffer_load_b128(krs, voff, (16 + i) * 2048, NT); SB(); }
    BARRIER();
    u32x4 q;
    {
      f32x2 s = {0.f, 0.f};
#pragma unroll
      for (int i = 0; i < 10; ++i) s += *(const f32x2*)(lds + L_PART + i * 512 + (8 * c + 2 * r) * 4);
      const unsigned v = pkbf(s[0], s[1]);
      auto a = __builtin_amdgcn_permlane16_swap(v, v, false, false);
      auto e = __builtin_amdgcn_permlane32_swap(a[0], a[0], false, false);
      auto o = __builtin_amdgcn_permlane32_swap(a[1], a[1], false, false);
      q = u32x4{e[0], o[0], e[1], o[1]};
    }
    float* sc = (float*)(lds + L_SC) + wave * 2048 + lane;
    float m, s;
    int sh = 0;
#define SHIFT() const unsigned tb = t0 + __builtin_amdgcn_readfirstlane(sh); sh = *(lds_int*)(lds + L_SH)
    m = s = kbatch<K_FIRST>(buf, ext, q, krs, vrs, voff, 0, c); sc[0] = s;
    s = kbatch<K_EXT>(buf, ext, q, krs, vrs, voff, 0, c); sc[64] = s; m = fmaxf(m, s);
    s = kbatch<K_RAMP>(buf, ext, q, krs, vrs, voff, 2 * 32768, c); sc[128] = s; m = fmaxf(m, s);
#pragma unroll 1
    for (int bt = 3; bt < 31; ++bt) {
      SHIFT();
      s = kbatch<K_LOOP>(buf, ext, q, krs, vrs, voff, bt * 32768, c, tb + (bt * p_tau >> 3));
      sc[bt * 64] = s; m = fmaxf(m, s);
    }
    s = kbatch<K_LAST>(buf, ext, q, krs, vrs, voff, 31 * 32768, c); sc[31 * 64] = s; m = fmaxf(m, s);
    s = sc[0];
    float s2 = sc[16 * 64];
    m = wave_max(m);
    float acc[8], l = 0.f;
#pragma unroll
    for (int j = 0; j < 8; ++j) acc[j] = 0.f;
#pragma unroll 1
    for (int bt = 0; bt < 16; ++bt) {
      const float e = __expf(s - m);
      SHIFT();
      l += e + __expf(s2 - m);
      s = sc[bt * 64 + 64];
      s2 = sc[((bt + 17) & 31) * 64];
      vbatch<false, 0, 16>(buf, acc, f2bf(e), vrs, voff, bt * 32768, tb + ((32 * p_tau + bt * p_tau2) >> 3));
    }
    l = wave_sum(l);
    if (lane == 0) *(f32x2*)(lds + L_ML + wave * 8) = f32x2{m, l};
#pragma unroll 1
    for (int bt = 16; bt < 31; ++bt) {
      const float e = __expf(s - m);
      SHIFT();
      s = sc[bt * 64 + 64];
      vbatch<false, 0, 16>(buf, acc, f2bf(e), vrs, voff, bt * 32768, tb + ((32 * p_tau + bt * p_tau2) >> 3));
    }
    {
      const unsigned pl = f2bf(__expf(s - m));
      vbatch<true, 0, 16 - DIST>(buf, acc, pl, vrs, voff, 31 * 32768);
      BARRIER();
      const f32x2 o = *(const f32x2*)(lds + L_ML + (wave ^ 1) * 8);
      const float M = fmaxf(m, o[0]), f0 = __expf(m - M);
      float f = f0 / (l * f0 + o[1] * __expf(o[0] - M));
      PIN(f); SB();
      vbatch<true, 16 - DIST, 16>(buf, acc, pl, vrs, voff, 31 * 32768);
      const f32x2 y = fold8to2(acc);
      *(unsigned*)(lds + L_OP + wave * 256 + (4 * c + r) * 4) = pkbf(y[0] * f, y[1] * f);
    }
    BARRIER();
    if (!ffn) {
      const int j = 2 * (id - NF) + wave, off = WS_R + b * 163840 + j * 8192;
      const float xres = asf((unsigned)x[b * D + 64 * j + 4 * c + 2 * (r & 1) + (r >> 1)] << 16);
      __builtin_amdgcn_s_sleep(21);
      const float hres = rbf(xres + rbf(gather<H>(wsr, lane * 16, off, r)));
      if (r < NREP) __builtin_amdgcn_raw_buffer_store_b64(pack_quad(hres), wsr, r * 1280 + c * 8, WS_GH + b * 5120 + j * 128, DEV_ST);
      __builtin_amdgcn_s_sleep(66);
      FENCE();
      const u32x2 y = pack_quad(rbf(hres + rbf(gather<NF>(wsr, lane * 16, off + 81920, r))));
      if (r == 0) __builtin_amdgcn_raw_buffer_store_b64(y, mkrsrc(out, B * D * 2), c * 8, b * D * 2 + j * 128, DEV_ST);
      return;
    }
    if (wave == 1) return;
    f32x2 cs = {0.f, 0.f};
#pragma unroll
    for (int i = 0; i < 10; ++i) cs += *(const f32x2*)(lds + L_CS + i * 512 + lane * 8);
    PIN(cs);
    BARRIER();
    const int gh = WS_GH + b * 5120 + (id % NREP) * 1280;
    __builtin_amdgcn_s_sleep(47);
    u32x4 ga = __builtin_amdgcn_raw_buffer_load_b128(wsr, lane * 16, gh, DEV_LD);
    unsigned gb = __builtin_amdgcn_raw_buffer_load_b32(wsr, lane * 4, gh + 1024, DEV_LD);
    FENCE();
    PIN(ga); PIN(gb);
    if (__any(missing(ga) || gb == EMPTY)) {
#pragma unroll 1
      for (int spin = 0; spin < SPIN_CAP; ++spin) {
        if (missing(ga)) ga = __builtin_amdgcn_raw_buffer_load_b128(wsr, lane * 16, gh, DEV_LD);
        if (gb == EMPTY) gb = __builtin_amdgcn_raw_buffer_load_b32(wsr, lane * 4, gh + 1024, DEV_LD);
        FENCE();
        if (!__any(missing(ga) || gb == EMPTY)) break;
        __builtin_amdgcn_s_sleep(1);
      }
    }
    *(u32x4*)(lds + L_H + lane * 16) = ga;
    *(unsigned*)(lds + L_H + 1024 + lane * 4) = gb;
    BARRIER();
    PIN(ga); PIN(gb);
    const unsigned g[5] = {ga[0], ga[1], ga[2], ga[3], gb};
    float t = 0.f;
#pragma unroll
    for (int i = 0; i < 5; ++i) t = dot2(g[i], 0x3f803f80u, t);
    const float mean = wave_sum(t) * (1.f / D);
    f32x2 dd = {0.f, 0.f};
#pragma unroll
    for (int i = 0; i < 5; ++i) { const f32x2 d = f32x2{asf(g[i] << 16), asf(g[i] & 0xffff0000u)} - mean; dd += d * d; }
    const float var = wave_sum(dd[0] + dd[1]) * (1.f / D);
    float rs = __builtin_amdgcn_rcpf(rbf(__builtin_amdgcn_sqrtf(rbf(rbf(var) + 1e-5f))));
    cs *= -rbf(mean);
    PIN(cs); PIN(rs);
    BARRIER();
    {
      f32x2 p[10];
#pragma unroll
      for (int i = 0; i < 10; ++i) p[i] = *(const f32x2*)(lds + L_PART + i * 512 + lane * 8);
      const f32x2 y = ((((p[0] + p[1]) + (p[2] + p[3])) + ((p[4] + p[5]) + (p[6] + p[7]))) + ((p[8] + p[9]) + cs)) * rs;
      const float gt = rbf(y[0]), up = rbf(y[1]);
      *(unsigned*)(lds + L_ACT + lane * 4) = f2bf(rbf(gt * __builtin_amdgcn_rcpf(1.f + __expf(-gt))) * up);
    }
    BARRIER();
    asm volatile(".Lpf_z:");
    return;
  }
  const int w = wave - 2;
  {
    const unsigned* x32 = (const unsigned*)(x + b * D);
    unsigned xa[5];
#pragma unroll
    for (int i = 0; i < 5; ++i) xa[i] = x32[lane + 64 * i];
        const float xo = asf((unsigned)x[b * D + 64 * w + 16 * (2 * (r & 1) + (c & 1)) + 2 * (((c >> 1) + b) & 7) + (r >> 1)] << 16);
    SB();
    const rsrc_t qrs = mkrsrc(wq, D * H * DH * 2);
    u32x8 wv[8];
#pragma unroll
    for (int i = 0; i < 8; ++i) {
      const int vo = (64 * w + 16 * r) * 8192 + id * 256 + c * 16, so = ((i + b) & 7) * 16384;
      const u32x4 v0 = __builtin_amdgcn_raw_buffer_load_b128(qrs, vo, so, 0); SB();
      const u32x4 v1 = __builtin_amdgcn_raw_buffer_load_b128(qrs, vo, so + 8192, 0); SB();
      wv[i] = __builtin_shufflevector(v0, v1, 0, 1, 2, 3, 4, 5, 6, 7);
    }
    BARRIER();
    float s = 0.f;
#pragma unroll
    for (int i = 0; i < 5; ++i) s = dot2(xa[i], 0x3f803f80u, s);
    const float mean = wave_sum(s) * (1.f / D);
    f32x2 dd = {0.f, 0.f};
#pragma unroll
    for (int i = 0; i < 5; ++i) { const f32x2 d = f32x2{asf(xa[i] << 16), asf(xa[i] & 0xffff0000u)} - mean; dd += d * d; }
    const float var = wave_sum(dd[0] + dd[1]) * (1.f / D);
    const float sd = rbf(sqrtf(rbf(rbf(var) + 1e-5f)));
    const unsigned xl = f2bf(rbf(xo - rbf(mean)) / sd);
    const unsigned m0 = (c & 4) ? 0u : ~0u, m1 = ~m0;
    u32x4 a[8];
#define QA(i) a[i] = u32x4{dppu<BC + 2 * i>(xl) & m0, dppu<BC + 2 * i>(xl) & m1, dppu<BC + 2 * i + 1>(xl) & m0, dppu<BC + 2 * i + 1>(xl) & m1}; PIN(a[i]);
    QA(0) QA(1) QA(2) QA(3) QA(4) QA(5) QA(6) QA(7)
#undef QA
    SB();
    f32x4 acc = {0.f, 0.f, 0.f, 0.f};
#pragma unroll
    for (int i = 0; i < 8; ++i) { acc = smf(a[i], wv[i], acc, (c & 3) * 0x5555); SB(); }
    if (lane < 32) *(f32x4*)(lds + L_PART + w * 512 + (8 * c + 4 * r) * 4) = acc;
  }
  BARRIER();
  const int idx1 = (c & 1) ? 0xeeee : 0x4444, idx4 = (c & 4) ? 0xeeee : 0x4444;
  int idxd = (2 * (c & 1) + ((c >> 1) & 1)) * 0x5555;
  PIN(idxd);
  const int fcol = 4 * c + 2 * (r & 1) + (r >> 1);
  u32x8 po[4][2], pg[2][2], pu[2][2];
  u32x2 rd[16];
  asm volatile("" : "+s"(wd));
  const rsrc_t drs = mkrsrc(wd, D * F * 2);
  const int vofu = (64 * w + 4 * r) * (F * 2) + id * 128 + c * 8, vofd = (64 * id + 2 * r) * (D * 2) + w * 128 + c * 8;
  const u32x4 ones = {0x3f803f80u, 0x3f803f80u, 0x3f803f80u, 0x3f803f80u};
  if (ffn) {
    load_tile<2, F * 2>(mkrsrc(wg, D * F * 2), vofu, pu);
    float cg = tile_col(pu, ones, ones, idx4, lane);
#pragma unroll
    for (int k = 0; k < 4; ++k) {
      const u32x8 v = pu[k >> 1][k & 1];
      *(u32x4*)(lds + L_GATE + (w * 8 + 2 * k) * 1024 + lane * 16) = __builtin_shufflevector(v, v, 0, 1, 2, 3);
      *(u32x4*)(lds + L_GATE + (w * 8 + 2 * k + 1) * 1024 + lane * 16) = __builtin_shufflevector(v, v, 4, 5, 6, 7);
    }
    PIN(cg); SB();
    load_tile<2, F * 2>(mkrsrc(wu, D * F * 2), vofu, pu);
    *(f32x2*)(lds + L_CS + w * 512 + fcol * 8) = f32x2{cg, tile_col(pu, ones, ones, idx4, lane)};
    SB();
  } else {
#pragma unroll
    for (int k = 0; k < 4; ++k) pu[k >> 1][k & 1] = u32x8{0u, 0u, 0u, 0u, 0u, 0u, 0u, 0u};
  }
  if (w == 0) {
    if (!ffn) {
#pragma unroll 1
      for (int i = 0; i < 4; ++i) __builtin_amdgcn_s_sleep(127);
    }
    const u64 token = *(const u64*)(lds + L_TOK);
    u64 v = token;
#pragma unroll 1
    for (int spin = 0; spin < SPIN_CAP; ++spin) {
      v = ld_agent(ws + WS_READY + b * RSTR + (lane & 31) * 8);
      if (__all(v >> 16 == token >> 16)) break;
      __builtin_amdgcn_s_sleep(64);
    }
    const int sh = (int)(wave_sum((float)(short)((unsigned)v - (unsigned)token)) * (1.f / 64));
    if (lane == 0) *(lds_int*)(lds + L_SH) = max(min(sh, 200), -200);
    if (id == 0 && lane == 0) st_agent(ws + WS_EPOCH + b * 256, token);
  }
  load_tile<4, D * 2>(mkrsrc(wo, H * DH * D * 2), (128 * id + 4 * r) * (D * 2) + w * 128 + c * 8, po);
  const int pub = WS_R + b * 163840 + (w * 32 + id) * 256;
  int aop = L_OP + (c & 2) * 128 + r * 16;
  PIN(aop);
  auto oproj = [&]() {
    f32x4 c0 = {0.f, 0.f, 0.f, 0.f}, c1 = c0;
    PIN(c0); PIN(c1);
    BARRIER();
    u32x4 a[4];
#pragma unroll
    for (int s = 0; s < 4; ++s) a[s] = *(const u32x4*)(lds + aop + s * 64);
    SB();
#pragma unroll
    for (int s = 0; s < 4; ++s) { c0 = smf(a[s], po[s][0], c0, idx1); c1 = smf(a[s], po[s][1], c1, idx1); }
    const f32x4 y = {c0[0] + c0[2], c0[1] + c0[3], c1[0] + c1[2], c1[1] + c1[3]};
    if (lane < 16) __builtin_amdgcn_raw_buffer_store_b128(__builtin_bit_cast(u32x4, y), wsr, c * 16, pub, DEV_ST);
    SB();
  };
  BARRIER();
  if (!ffn) { oproj(); return; }
#pragma unroll
  for (int i = 0; i < DPOS; ++i) { rd[i] = __builtin_amdgcn_raw_buffer_load_b64(drs, vofd, (16 * (i >> 2) + 4 * (i & 2) + (i & 1)) * D * 2, 0); SB(); }
  oproj();
  BARRIER(); SB();
#pragma unroll
  for (int i = DPOS; i < 16; ++i) { rd[i] = __builtin_amdgcn_raw_buffer_load_b64(drs, vofd, (16 * (i >> 2) + 4 * (i & 2) + (i & 1)) * D * 2, 0); SB(); if (i < 15) __builtin_amdgcn_s_sleep(2); SB(); }
#pragma unroll
  for (int k = 0; k < 4; ++k)
    pg[k >> 1][k & 1] = __builtin_shufflevector(*(const u32x4*)(lds + L_GATE + (w * 8 + 2 * k) * 1024 + lane * 16),
                                                *(const u32x4*)(lds + L_GATE + (w * 8 + 2 * k + 1) * 1024 + lane * 16), 0, 1, 2, 3, 4, 5, 6, 7);
  f32x4 g0 = {0.f, 0.f, 0.f, 0.f}, g1 = g0, u0 = g0, u1 = g0;
  int ah = L_H + w * 128 + r * 16, ap = L_PART + w * 512 + fcol * 8;
  PIN(g0); PIN(g1); PIN(u0); PIN(u1); PIN(ah); PIN(ap);
  BARRIER();
  {
    const u32x4 a0 = *(const u32x4*)(lds + ah), a1 = *(const u32x4*)(lds + ah + 64);
    SB();
    g0 = smf(a0, pg[0][0], g0, idx4); g1 = smf(a0, pg[0][1], g1, idx4); u0 = smf(a0, pu[0][0], u0, idx4); u1 = smf(a0, pu[0][1], u1, idx4);
    SB();
    g0 = smf(a1, pg[1][0], g0, idx4); g1 = smf(a1, pg[1][1], g1, idx4); u0 = smf(a1, pu[1][0], u0, idx4); u1 = smf(a1, pu[1][1], u1, idx4);
    *(f32x2*)(lds + ap) = f32x2{lane & 32 ? g1[0] : g0[0], lane & 32 ? u1[0] : u0[0]};
  }
  SB();
  BARRIER();
  SB();
  f32x4 c0 = {0.f, 0.f, 0.f, 0.f}, c1 = c0;
  int aa = L_ACT + r * 16;
  PIN(c0); PIN(c1); PIN(aa);
  BARRIER();
  u32x4 a[4];
#pragma unroll
  for (int k = 0; k < 4; ++k) a[k] = *(const u32x4*)(lds + aa + 64 * k);
  SB();
#define DB(k) __builtin_shufflevector(__builtin_shufflevector(rd[4 * k], rd[4 * k + 1], 0, 1, 2, 3), __builtin_shufflevector(rd[4 * k + 2], rd[4 * k + 3], 0, 1, 2, 3), 0, 1, 2, 3, 4, 5, 6, 7)
  c0 = smf(a[0], DB(0), c0, idxd); c1 = smf(a[1], DB(1), c1, idxd);
  c0 = smf(a[2], DB(2), c0, idxd); c1 = smf(a[3], DB(3), c1, idxd);
  const f32x4 y = c0 + c1;
  if (lane < 16) __builtin_amdgcn_raw_buffer_store_b128(__builtin_bit_cast(u32x4, y), wsr, c * 16, pub + 81920, DEV_ST);
}
}

extern "C" void kernel_launch(void* const* d_in, const int* in_sizes, int n_in, void* d_out, int out_size, void* d_ws, size_t ws_size,
                              hipStream_t stream) {
  fused_layer<<<256, 768, 0, stream>>>((const unsigned short*)d_in[0], (const unsigned short*)d_in[1], (const unsigned short*)d_in[2],
                                       (const unsigned short*)d_in[3], (const unsigned short*)d_in[4], (const unsigned short*)d_in[6],
                                       (const unsigned short*)d_in[7], (const unsigned short*)d_in[8], (unsigned short*)d_out, (char*)d_ws);
}
```
